```python
import torch

def _softmax(x, axis=-1):
    return torch.softmax(x, dim=axis)
def _gelu(x):
    return torch.nn.functional.gelu(x, approximate="tanh")
def _silu(x):
    return torch.nn.functional.silu(x)
def _sigmoid(x):
    return torch.sigmoid(x)
def _layernorm(x, axis=-1, eps=1e-5):
    m = x.mean(dim=axis, keepdim=True)
    v = x.var(dim=axis, keepdim=True, unbiased=False)
    return (x - m) / torch.sqrt(v + eps)
def _scatter_add(tgt, idx, src, axis=0):
    return tgt.clone().index_add_(axis, idx, src)
def _segment_sum(src, seg, n):
    out = torch.zeros((n,) + src.shape[1:], dtype=src.dtype)
    return out.index_add_(0, seg, src)
def _pad(x, pw):
    flat = tuple(v for pair in reversed(pw) for v in pair)
    return torch.nn.functional.pad(x, flat)

kernel_name = 'parallel_sum__linear_kernel_hedgehog__geglu_ffn__prompt'

def setup_inputs():

    _w = (lambda a: (lambda t: t.to(torch.bfloat16) if t.is_floating_point() else t)(torch.as_tensor(a)))
    def _A_setup():
        B, S, D, H = 1, 2048, 1024, 32
        Dh = D // H
        return {
            "x": _w((lambda *s: torch.randn(*s, dtype=torch.bfloat16) * 0.05)(B, S, D)),
            "wqkv": _w((lambda *s: torch.randn(*s, dtype=torch.bfloat16) * 0.8)(D, 3 * D)),
            "w_phi": _w((lambda *s: torch.randn(*s, dtype=torch.bfloat16) * 0.8)(H, Dh, Dh) * (Dh ** -0.5)),
            "wo": _w((lambda *s: torch.randn(*s, dtype=torch.bfloat16) * 0.2)(D, D)),
        }
    _A_in = _A_setup()

    def _B_setup():
        B, S, D, F = 1, 2048, 1024, 3584
        return {
            "x": _w((lambda *s: torch.randn(*s, dtype=torch.bfloat16) * 0.05)(B, S, D)),
            "w_gate": _w((lambda *s: torch.randn(*s, dtype=torch.bfloat16) * 0.05)(D, F)),
            "w_up": _w((lambda *s: torch.randn(*s, dtype=torch.bfloat16) * 0.05)(D, F)),
            "w_down": _w((lambda *s: torch.randn(*s, dtype=torch.bfloat16) * 0.05)(F, D)),
        }
    _B_in = _B_setup()

    return {
        "A_x": _A_in["x"],
        "A_wqkv": _A_in["wqkv"],
        "A_w_phi": _A_in["w_phi"],
        "A_wo": _A_in["wo"],
        "B_x": _B_in["x"],
        "B_w_gate": _B_in["w_gate"],
        "B_w_up": _B_in["w_up"],
        "B_w_down": _B_in["w_down"],
    }

def reference(A_x, A_wqkv, A_w_phi, A_wo, B_x, B_w_gate, B_w_up, B_w_down):
    def _A_ref(x, wqkv, w_phi, wo):
        B, S, D = x.shape
        H = 32; Dh = D // H
        qkv = (lambda x, *d: x.permute(*d))((x @ wqkv).reshape(B, S, 3, H, Dh), 2, 0, 3, 1, 4)
        q, k, v = qkv[0], qkv[1], qkv[2]
        pq = torch.einsum("bhsd,hdf->bhsf", q, w_phi)
        pk = torch.einsum("bhsd,hdf->bhsf", k, w_phi)
        phi_q = torch.concatenate([_softmax(pq, axis=-1), _softmax(-pq, axis=-1)], axis=-1)
        phi_k = torch.concatenate([_softmax(pk, axis=-1), _softmax(-pk, axis=-1)], axis=-1)
        kv = torch.einsum("bhsf,bhsd->bhfd", phi_k, v)
        z  = torch.einsum("bhsf->bhf", phi_k)
        num = torch.einsum("bhqf,bhfd->bhqd", phi_q, kv)
        den = torch.einsum("bhqf,bhf->bhq", phi_q, z)[..., None] + 1e-6
        o = num / den
        return (lambda x, *d: x.permute(*d))(o, 0, 2, 1, 3).reshape(B, S, D) @ wo

    def _B_ref(x, w_gate, w_up, w_down):
        return (_gelu(x @ w_gate) * (x @ w_up)) @ w_down

    x = A_x
    xn = _layernorm(x, axis=-1)
    return x + _A_ref(xn, A_wqkv, A_w_phi, A_wo) + _B_ref(xn, B_w_gate, B_w_up, B_w_down)
```

```cpp
#include <hip/hip_runtime.h>
#include <hip/hip_bf16.h>
typedef unsigned short u16;
typedef short bf16x8 __attribute__((ext_vector_type(8)));
typedef float f32x4 __attribute__((ext_vector_type(4)));
__device__ __forceinline__ float bf2f(u16 v){ return __uint_as_float(((unsigned)v)<<16); }
__device__ __forceinline__ u16 f2bf(float f){ unsigned u=__float_as_uint(f); u += 0x7fffu + ((u>>16)&1u); return (u16)(u>>16); }
constexpr int S=2048, D=1024, H=32, DH=32, F=3584;

__global__ void ln_kernel(const u16* x, u16* xn){
  int row=blockIdx.x; int t=threadIdx.x;
  __shared__ float red[256];
  float v[4]; float s=0;
  for(int i=0;i<4;i++){ v[i]=bf2f(x[row*D+t*4+i]); s+=v[i]; }
  red[t]=s; __syncthreads();
  for(int o=128;o>0;o>>=1){ if(t<o) red[t]+=red[t+o]; __syncthreads(); }
  float m=red[0]/D; __syncthreads();
  float q=0; for(int i=0;i<4;i++){ float d=v[i]-m; q+=d*d; }
  red[t]=q; __syncthreads();
  for(int o=128;o>0;o>>=1){ if(t<o) red[t]+=red[t+o]; __syncthreads(); }
  float var=red[0]/D; float r=1.0f/sqrtf(var+1e-5f);
  for(int i=0;i<4;i++) xn[row*D+t*4+i]=f2bf((v[i]-m)*r);
}
__global__ void gemm_naive(const u16* A,int lda,const u16* B,int ldb,float* C,int ldc,int K){
  int wid=threadIdx.x>>6, lane=threadIdx.x&63;
  int r0=blockIdx.y*64+wid*16, c0=blockIdx.x*16;
  f32x4 acc={0,0,0,0};
  for(int k=0;k<K;k+=32){
    bf16x8 a=*(const bf16x8*)(A+(size_t)(r0+(lane&15))*lda+k+8*(lane>>4));
    bf16x8 b; for(int j=0;j<8;j++) b[j]=(short)B[(size_t)(k+8*(lane>>4)+j)*ldb+c0+(lane&15)];
    acc=__builtin_amdgcn_mfma_f32_16x16x32_bf16(a,b,acc,0,0,0);
  }
  for(int j=0;j<4;j++) C[(size_t)(r0+(lane>>4)*4+j)*ldc+c0+(lane&15)]=acc[j];
}
__global__ void phi_kernel(const float* qkv,const u16* wphi,float* phi  ){
  int row=blockIdx.x; int which=blockIdx.y; int t=threadIdx.x;
  int h=t>>5,f=t&31;
  __shared__ float q[1024];
  q[t]=bf2f(f2bf(qkv[(size_t)row*3072+which*1024+t])); __syncthreads();
  float p=0; for(int d=0;d<32;d++) p+=q[h*32+d]*bf2f(wphi[(h*32+d)*32+f]);
  float mx=p,mn=p; for(int o=16;o>0;o>>=1){ mx=fmaxf(mx,__shfl_xor(mx,o)); mn=fminf(mn,__shfl_xor(mn,o)); }
  float e1=__expf(p-mx), e2=__expf(mn-p); float s1=e1,s2=e2;
  for(int o=16;o>0;o>>=1){ s1+=__shfl_xor(s1,o); s2+=__shfl_xor(s2,o);}
  float* o_=phi+(((size_t)which*S+row)*H+h)*64;
  o_[f]=e1/s1; o_[32+f]=e2/s2;
}
__global__ void kv_kernel(const float* phi,const float* qkv,float* kv  ){
  int h=blockIdx.x; int f=blockIdx.y; int d=threadIdx.x;
  if(d>32) return;
  const float* pk=phi+(size_t)S*H*64;
  float a=0; for(int s=0;s<S;s++){ float p=pk[((size_t)s*H+h)*64+f]; float v= d<32? bf2f(f2bf(qkv[(size_t)s*3072+2048+h*32+d])):1.0f; a+=p*v; }
  kv[(h*64+f)*33+d]=a;
}
__global__ void o_kernel(const float* phi,const float* kv,u16* o){
  int row=blockIdx.x; int t=threadIdx.x; int h=t>>5,d=t&31;
  const float* pq=phi+((size_t)row*H+h)*64; float n=0,den=0;
  for(int f=0;f<64;f++){ n+=pq[f]*kv[(h*64+f)*33+d]; den+=pq[f]*kv[(h*64+f)*33+32]; }
  o[(size_t)row*D+t]=f2bf(n/(den+1e-6f));
}
__global__ void geglu_kernel(const float* g,const float* u,u16* h,int n){
  int i=blockIdx.x*256+threadIdx.x; if(i>=n) return;
  float x=g[i]; float t=tanhf(0.7978845608f*(x+0.044715f*x*x*x)); h[i]=f2bf(0.5f*x*(1.f+t)*u[i]);
}
__global__ void final_kernel(const u16* x,const float* a,const float* b,u16* out,int n){
  int i=blockIdx.x*256+threadIdx.x; if(i>=n) return; out[i]=f2bf(bf2f(x[i])+a[i]+b[i]);
}
extern "C" void kernel_launch(void* const* d_in,const int* in_sizes,int n_in,void* d_out,int out_size,void* d_ws,size_t ws_size,hipStream_t stream){
  const u16* x=(const u16*)d_in[0]; const u16* wqkv=(const u16*)d_in[1]; const u16* wphi=(const u16*)d_in[2]; const u16* wo=(const u16*)d_in[3];
  const u16* wg=(const u16*)d_in[5]; const u16* wu=(const u16*)d_in[6]; const u16* wd=(const u16*)d_in[7];
  char* ws=(char*)d_ws; size_t off=0; auto al=[&](size_t b){ char* p=ws+off; off+=(b+255)/256*256; return p; };
  u16* xn=(u16*)al((size_t)S*D*2); float* qkv=(float*)al((size_t)S*3072*4); float* phi=(float*)al((size_t)2*S*H*64*4);
  float* kv=(float*)al(H*64*33*4); u16* o=(u16*)al((size_t)S*D*2); float* g=(float*)al((size_t)S*F*4); float* u=(float*)al((size_t)S*F*4);
  u16* h=(u16*)al((size_t)S*F*2); float* a=(float*)al((size_t)S*D*4); float* b=(float*)al((size_t)S*D*4);
  ln_kernel<<<S,256,0,stream>>>(x,xn);
  gemm_naive<<<dim3(3072/16,S/64),256,0,stream>>>(xn,D,wqkv,3072,qkv,3072,D);
  phi_kernel<<<dim3(S,2),1024,0,stream>>>(qkv,wphi,phi);
  kv_kernel<<<dim3(H,64),64,0,stream>>>(phi,qkv,kv);
  o_kernel<<<S,1024,0,stream>>>(phi,kv,o);
  gemm_naive<<<dim3(D/16,S/64),256,0,stream>>>(o,D,wo,D,a,D,D);
  gemm_naive<<<dim3(F/16,S/64),256,0,stream>>>(xn,D,wg,F,g,F,D);
  gemm_naive<<<dim3(F/16,S/64),256,0,stream>>>(xn,D,wu,F,u,F,D);
  geglu_kernel<<<(S*F+255)/256,256,0,stream>>>(g,u,h,S*F);
  gemm_naive<<<dim3(D/16,S/64),256,0,stream>>>(h,F,wd,D,b,D,F);
  final_kernel<<<(S*D+255)/256,256,0,stream>>>(x,a,b,(u16*)d_out,S*D);
}
```

```cpp
#include <hip/hip_runtime.h>
#include <hip/hip_bf16.h>

#define DI __device__ __forceinline__
#define GAS __attribute__((address_space(1)))
#define LAS __attribute__((address_space(3)))
typedef unsigned short u16;
typedef short bf16x8 __attribute__((ext_vector_type(8)));
typedef short s16x4 __attribute__((ext_vector_type(4)));
typedef float f32x2 __attribute__((ext_vector_type(2)));
typedef float f32x4 __attribute__((ext_vector_type(4)));
typedef float f32x16 __attribute__((ext_vector_type(16)));
typedef __bf16 bf16x2_t __attribute__((ext_vector_type(2)));
typedef unsigned u32x2 __attribute__((ext_vector_type(2)));
typedef unsigned u32x4 __attribute__((ext_vector_type(4)));
typedef unsigned u32x8 __attribute__((ext_vector_type(8)));
typedef unsigned u32x16 __attribute__((ext_vector_type(16)));

constexpr int S_ = 2048, D_ = 1024, F_ = 3584;
constexpr size_t WS_FLAGS = 0;
constexpr size_t WS_XN = 65536;
constexpr size_t WS_A2 = WS_XN + (4u << 20);
constexpr size_t A2_BYTES = (size_t)144 * 131072;
constexpr size_t WS_KVP = WS_A2 + A2_BYTES;
constexpr size_t WS_EX = WS_KVP + (2u << 20);
constexpr int FL_TMO = 0, FL_HX = 256, FL_O = 512, FL_HY = 768, FL_EX = 1024;
constexpr unsigned SENT = 0xffffffffu;
constexpr unsigned KVP_BYTES = (1u << 20) + 131072u;
constexpr unsigned SPIN_CAP = 1u << 18;
#define OUT_AUX 16
#define A2_AUX 16

DI unsigned rfl(unsigned x) { return __builtin_amdgcn_readfirstlane(x); }
DI unsigned cvtpk(float lo, float hi) { f32x2 v = {lo, hi}; bf16x2_t b = __builtin_convertvector(v, bf16x2_t); return __builtin_bit_cast(unsigned, b); }
DI float bflo(unsigned w) { return __uint_as_float(w << 16); }
DI float bfhi(unsigned w) { return __uint_as_float(w & 0xffff0000u); }
DI float rbf(float x) { return bflo(cvtpk(x, x)); }
DI u32x4 mksrd(const void* p, unsigned bytes) {
  const unsigned long long a = (unsigned long long)p;
  u32x4 r; r[0] = rfl((unsigned)a); r[1] = rfl((unsigned)(a >> 32) & 0xffffu); r[2] = rfl(bytes); r[3] = 0x00020000u; return r;
}
template <int OP> DI float op2(float a, float b) { if constexpr (OP == 0) return fmaxf(a, b); else if constexpr (OP == 1) return fminf(a, b); else return a + b; }
template <int OP> DI float rowred(float x) {
  auto a = __builtin_amdgcn_permlane16_swap(__float_as_uint(x), __float_as_uint(x), false, false);
  x = op2<OP>(__uint_as_float(a[0]), __uint_as_float(a[1]));
  auto b = __builtin_amdgcn_permlane32_swap(__float_as_uint(x), __float_as_uint(x), false, false);
  return op2<OP>(__uint_as_float(b[0]), __uint_as_float(b[1]));
}
DI bf16x8 tr_frag(unsigned a0, unsigned a1) {
  s16x4 lo = __builtin_amdgcn_ds_read_tr16_b64_v4i16((LAS s16x4*)(size_t)a0);
  s16x4 hi = __builtin_amdgcn_ds_read_tr16_b64_v4i16((LAS s16x4*)(size_t)a1);
  return __builtin_shufflevector(lo, hi, 0, 1, 2, 3, 4, 5, 6, 7);
}
DI bf16x8 mk8(unsigned a, unsigned b, unsigned c, unsigned d) { u32x4 w = {a, b, c, d}; return __builtin_bit_cast(bf16x8, w); }
#define MFMA16(a, b, c) __builtin_amdgcn_mfma_f32_16x16x32_bf16((a), (b), (c), 0, 0, 0)
DI bool spin_give_up(unsigned& spins, GAS unsigned* tmo) {
  __builtin_amdgcn_s_sleep(2);
  if (++spins > SPIN_CAP) { __hip_atomic_store(tmo, 1u, __ATOMIC_RELAXED, __HIP_MEMORY_SCOPE_AGENT); return true; }
  return (spins & 255u) == 0u && __builtin_amdgcn_readfirstlane(__hip_atomic_load(tmo, __ATOMIC_RELAXED, __HIP_MEMORY_SCOPE_AGENT));
}
template <int CTRL> DI float dppadd(float x) { return x + __uint_as_float(__builtin_amdgcn_update_dpp(0u, __float_as_uint(x), CTRL, 0xf, 0xf, true)); }
DI float wave_sum(float x) {
  x = dppadd<0xb1>(x); x = dppadd<0x4e>(x); x = dppadd<0x141>(x); x = dppadd<0x140>(x);
  return rowred<2>(x);
}

#define K1_AUX 17
struct K1Args { const u16* x; const u16* wqkv; const u16* wphi; const u16* wg; const u16* wu; const u16* wo; const u16* wd; const u16* out; char* ws; };
__global__ __launch_bounds__(512) void k1_ln(K1Args P) {
  const u16* __restrict__ x = P.x; char* __restrict__ ws = P.ws;
  __shared__ __attribute__((aligned(16))) char junk[8][1024];
  __shared__ __attribute__((aligned(16))) u16 sm[8][1024 + 8];
  const int tid = threadIdx.x, lane = tid & 63, w = tid >> 6;
  if (blockIdx.x >= 256) {
    const int e = blockIdx.x - 256, wv = __builtin_amdgcn_readfirstlane(w), gw = (e >> 3) * 8 + wv;
    const unsigned m0v = __builtin_amdgcn_readfirstlane((unsigned)(size_t)(LAS char*)&junk[0][0] + wv * 1024);
#define K1_TOUCH4(P) asm volatile("s_mov_b32 m0, %1\n\ts_nop 0\n\tglobal_load_lds_dwordx4 %0, off" :: "v"(P), "s"(m0v) : "memory")
#define K1_TOUCH1(P) asm volatile("s_mov_b32 m0, %1\n\ts_nop 0\n\tglobal_load_lds_dword %0, off" :: "v"(P), "s"(m0v) : "memory")
    unsigned long long code;
    asm volatile("s_getpc_b64 s[20:21]\n\ts_add_u32 s20, s20, k2_gemm1@rel32@lo+4\n\ts_addc_u32 s21, s21, k2_gemm1@rel32@hi+12" : "={s[20:21]}"(code) :: "scc");
    K1_TOUCH4((const char*)code + gw * 1024 + lane * 16);
    if (gw < 6) K1_TOUCH4((const char*)code + (32 + gw) * 1024 + lane * 16);
    if (gw == 4) K1_TOUCH1((const char*)((unsigned long long)__builtin_amdgcn_kernarg_segment_ptr() & ~4095ull) + lane * 64);
    if (gw >= 16 && gw < 21) {
      int u = (gw - 16) * 64 + lane;
      const char* p = (const char*)P.wqkv;
      if (u >= 96) { u -= 96; p = (const char*)P.wg; if (u >= 112) { u -= 112; p = (const char*)P.wu; } }
      K1_TOUCH1(p + (size_t)u * 65536);
    }
#define K1_PF 0
    constexpr int NQ = K1_PF * 192, NG = K1_PF * 224;
    for (int pc = e * 8 + wv; pc < NQ + 2 * NG + 64; pc += 256) {
      const char* p = pc < NQ ? (const char*)P.wqkv + (size_t)pc * 1024 : (pc < NQ + NG ? (const char*)P.wg + (size_t)(pc - NQ) * 1024 : (pc < NQ + 2 * NG ? (const char*)P.wu + (size_t)(pc - NQ - NG) * 1024 : (const char*)P.wphi + (size_t)(pc - NQ - 2 * NG) * 1024));
      K1_TOUCH4(p + lane * 16);
    }
    return;
  }
  const int row = blockIdx.x * 8 + w;
  const u32x4 a = *(const u32x4*)(x + (size_t)row * D_ + lane * 8);
  const u32x4 b = *(const u32x4*)(x + (size_t)row * D_ + 512 + lane * 8);
  if (tid < 288) *(u32x4*)(ws + WS_KVP + blockIdx.x * 4608 + tid * 16) = (u32x4){SENT, SENT, SENT, SENT};
  if (tid >= 288 && tid < 296) *(u32x4*)(ws + WS_FLAGS + blockIdx.x * 128 + (tid - 288) * 16) = (u32x4){0u, 0u, 0u, 0u};
  float v[16];
#pragma unroll
  for (int i = 0; i < 4; ++i) { v[2 * i] = bflo(a[i]); v[2 * i + 1] = bfhi(a[i]); v[8 + 2 * i] = bflo(b[i]); v[8 + 2 * i + 1] = bfhi(b[i]); }
  float s = 0.f;
#pragma unroll
  for (int i = 0; i < 16; ++i) s += v[i];
  const float mean = wave_sum(s) * (1.0f / D_);
  float q = 0.f;
#pragma unroll
  for (int i = 0; i < 16; ++i) { const float d = v[i] - mean; q += d * d; }
  const float mb = rbf(mean), vb = rbf(wave_sum(q) * (1.0f / D_));
  const float sd = rbf(__fsqrt_rn(rbf(vb + 1e-5f)));
  const float rs = __fdiv_rn(1.0f, sd);
  auto dv = [&](float xv) { const float d = rbf(xv - mb), q0 = d * rs; return __builtin_fmaf(__builtin_fmaf(-q0, sd, d), rs, q0); };
  u32x4 oa, ob;
#pragma unroll
  for (int i = 0; i < 4; ++i) { oa[i] = cvtpk(dv(v[2 * i]), dv(v[2 * i + 1])); ob[i] = cvtpk(dv(v[8 + 2 * i]), dv(v[8 + 2 * i + 1])); }
  *(u32x4*)&sm[w][lane * 8] = oa;
  *(u32x4*)&sm[w][512 + lane * 8] = ob;
  __syncthreads();
  const __amdgpu_buffer_rsrc_t rX = __builtin_amdgcn_make_buffer_rsrc(ws + WS_XN, 0, 4 << 20, 0x00020000);
  const int t = tid & 7;
#pragma unroll
  for (int i = 0; i < 2; ++i) {
    const int c = (tid >> 3) + 64 * i, bk = c >> 2, g = c & 3;
    const u32x2 lo = *(const u32x2*)&sm[t][32 * bk + 4 * g];
    const u32x2 hi = *(const u32x2*)&sm[t][32 * bk + 16 + 4 * g];
    const int token = blockIdx.x * 8 + t;
    __builtin_amdgcn_raw_buffer_store_b128((u32x4){lo[0], lo[1], hi[0], hi[1]}, rX, ((bk * 128 + (token >> 4)) * 64 + g * 16 + (token & 15)) * 16, 0, K1_AUX);
  }
}

constexpr int K2_LDS = 163840;
constexpr int K2_WPHI = 147456;
constexpr int K2_KVF = 151552;
struct K2Args { const u16* wqkv; const u16* wphi; const u16* wg; const u16* wu; const u16* wo; const u16* wd; const u16* x; u16* out; char* ws; };

DI float vmax(float a, float b) { float r; asm("v_max_f32_e32 %0, %1, %2" : "=v"(r) : "v"(a), "v"(b)); return r; }
DI float vmin(float a, float b) { float r; asm("v_min_f32_e32 %0, %1, %2" : "=v"(r) : "v"(a), "v"(b)); return r; }
DI float max3f(float a, float b, float c) { float r; asm("v_max3_f32 %0, %1, %2, %3" : "=v"(r) : "v"(a), "v"(b), "v"(c)); return r; }
DI float min3f(float a, float b, float c) { float r; asm("v_min3_f32 %0, %1, %2, %3" : "=v"(r) : "v"(a), "v"(b), "v"(c)); return r; }
template <int OP> DI float opx(float a, float b) { if constexpr (OP == 0) return vmax(a, b); else if constexpr (OP == 1) return vmin(a, b); else return a + b; }
template <int OP> DI void rowred2(float& a, float& b) {
  auto s = __builtin_amdgcn_permlane16_swap(__float_as_uint(a), __float_as_uint(b), false, false);
  const float m = opx<OP>(__uint_as_float(s[0]), __uint_as_float(s[1]));
  auto t = __builtin_amdgcn_permlane32_swap(__float_as_uint(m), __float_as_uint(m), false, false);
  const float r = opx<OP>(__uint_as_float(t[0]), __uint_as_float(t[1]));
  auto u = __builtin_amdgcn_permlane16_swap(__float_as_uint(r), __float_as_uint(r), false, false);
  a = __uint_as_float(u[0]); b = __uint_as_float(u[1]);
}
struct Phi { unsigned p[4], n[4]; };
DI float rbf1(float x) { return __uint_as_float(cvtpk(0.0f, x)); }
template <bool NORM>
DI void feature_map2(const f32x4& a0, const f32x4& a1, const f32x4& b0, const f32x4& b1, const bf16x8& w0, const bf16x8& w1, Phi& pa, Phi& pb) {
  const f32x4 z = {0.f, 0.f, 0.f, 0.f};
  const bf16x8 qa = mk8(cvtpk(a0[0], a0[1]), cvtpk(a0[2], a0[3]), cvtpk(a1[0], a1[1]), cvtpk(a1[2], a1[3]));
  const bf16x8 qb = mk8(cvtpk(b0[0], b0[1]), cvtpk(b0[2], b0[3]), cvtpk(b1[0], b1[1]), cvtpk(b1[2], b1[3]));
  const f32x4 pa0 = MFMA16(w0, qa, z), pa1 = MFMA16(w1, qa, z), pb0 = MFMA16(w0, qb, z), pb1 = MFMA16(w1, qb, z);
  const f32x2 xa[4] = {{rbf1(pa0[0]), rbf1(pa0[1])}, {rbf1(pa0[2]), rbf1(pa0[3])}, {rbf1(pa1[0]), rbf1(pa1[1])}, {rbf1(pa1[2]), rbf1(pa1[3])}};
  const f32x2 xb[4] = {{rbf1(pb0[0]), rbf1(pb0[1])}, {rbf1(pb0[2]), rbf1(pb0[3])}, {rbf1(pb1[0]), rbf1(pb1[1])}, {rbf1(pb1[2]), rbf1(pb1[3])}};
  float mxa = vmax(max3f(max3f(max3f(xa[0][0], xa[0][1], xa[1][0]), xa[1][1], xa[2][0]), xa[2][1], xa[3][0]), xa[3][1]);
  float mxb = vmax(max3f(max3f(max3f(xb[0][0], xb[0][1], xb[1][0]), xb[1][1], xb[2][0]), xb[2][1], xb[3][0]), xb[3][1]);
  float mna = vmin(min3f(min3f(min3f(xa[0][0], xa[0][1], xa[1][0]), xa[1][1], xa[2][0]), xa[2][1], xa[3][0]), xa[3][1]);
  float mnb = vmin(min3f(min3f(min3f(xb[0][0], xb[0][1], xb[1][0]), xb[1][1], xb[2][0]), xb[2][1], xb[3][0]), xb[3][1]);
  rowred2<0>(mxa, mxb); rowred2<1>(mna, mnb);
  constexpr float L2E = 1.4426950408889634f;
  const f32x2 lp = {L2E, L2E}, ln = {-L2E, -L2E};
  const f32x2 cpa = (f32x2){mxa, mxa} * ln, cna = (f32x2){mna, mna} * lp, cpb = (f32x2){mxb, mxb} * ln, cnb = (f32x2){mnb, mnb} * lp;
  f32x2 epa[4], ena[4], epb[4], enb[4];
#pragma unroll
  for (int i = 0; i < 4; ++i) {
    const f32x2 t0 = xa[i] * lp + cpa, t1 = xa[i] * ln + cna, t2 = xb[i] * lp + cpb, t3 = xb[i] * ln + cnb;
    epa[i] = (f32x2){__builtin_amdgcn_exp2f(t0[0]), __builtin_amdgcn_exp2f(t0[1])};
    ena[i] = (f32x2){__builtin_amdgcn_exp2f(t1[0]), __builtin_amdgcn_exp2f(t1[1])};
    epb[i] = (f32x2){__builtin_amdgcn_exp2f(t2[0]), __builtin_amdgcn_exp2f(t2[1])};
    enb[i] = (f32x2){__builtin_amdgcn_exp2f(t3[0]), __builtin_amdgcn_exp2f(t3[1])};
  }
  if constexpr (NORM) {
    const f32x2 spa2 = (epa[0] + epa[1]) + (epa[2] + epa[3]), sna2 = (ena[0] + ena[1]) + (ena[2] + ena[3]);
    const f32x2 spb2 = (epb[0] + epb[1]) + (epb[2] + epb[3]), snb2 = (enb[0] + enb[1]) + (enb[2] + enb[3]);
    float spa = spa2[0] + spa2[1], sna = sna2[0] + sna2[1], spb = spb2[0] + spb2[1], snb = snb2[0] + snb2[1];
    rowred2<2>(spa, spb); rowred2<2>(sna, snb);
    const float rpa = __builtin_amdgcn_rcpf(spa), rna = __builtin_amdgcn_rcpf(sna), rpb = __builtin_amdgcn_rcpf(spb), rnb = __builtin_amdgcn_rcpf(snb);
#pragma unroll
    for (int i = 0; i < 4; ++i) {
      epa[i] *= (f32x2){rpa, rpa}; ena[i] *= (f32x2){rna, rna}; epb[i] *= (f32x2){rpb, rpb}; enb[i] *= (f32x2){rnb, rnb};
    }
  }
#pragma unroll
  for (int i = 0; i < 4; ++i) {
    pa.p[i] = cvtpk(epa[i][0], epa[i][1]); pa.n[i] = cvtpk(ena[i][0], ena[i][1]); pb.p[i] = cvtpk(epb[i][0], epb[i][1]); pb.n[i] = cvtpk(enb[i][0], enb[i][1]);
  }
}
DI unsigned geglu2(f32x2 g, f32x2 u) {
  const f32x2 c0 = {-2.302208198f, -2.302208198f}, c1 = {-0.1029432397f, -0.1029432397f}, one = {1.0f, 1.0f};
  const f32x2 a = g * ((g * g) * c1 + c0);
  const f32x2 d = (f32x2){__builtin_amdgcn_exp2f(a[0]), __builtin_amdgcn_exp2f(a[1])} + one;
  const f32x2 h = (g * u) * (f32x2){__builtin_amdgcn_rcpf(d[0]), __builtin_amdgcn_rcpf(d[1])};
  return cvtpk(h[0], h[1]);
}
DI u32x2 geglu4(const f32x4& g, const f32x4& u) {
  return (u32x2){geglu2((f32x2){g[0], g[1]}, (f32x2){u[0], u[1]}), geglu2((f32x2){g[2], g[3]}, (f32x2){u[2], u[3]})};
}
DI f32x4 sub4(const f32x16& a, int tt) { return (f32x4){a[4 * tt], a[4 * tt + 1], a[4 * tt + 2], a[4 * tt + 3]}; }

constexpr int K3_LDS = 2 * 128 * 528;

DI void k3_phase(const u16* Ax, const u16* Awo, const u16* Awd, u16* Aout, char* ws, const unsigned lb, unsigned hseen) {
  int tid = threadIdx.x; asm volatile("" : "+v"(tid));
  const int lane = tid & 63;
  const int wid = rfl(tid >> 6), kg = wid >> 2, w = wid & 3;
  const int bid = blockIdx.x, xcd = bid & 7, idx = bid >> 3;
#define MIX 1
  const int q16 = (idx & 7) + 8 * (idx >> 4);
  const int kh = MIX ? ((idx >> 3) & 1) ^ 1 : (xcd >> 2) ^ 1;
  const int rt = MIX ? (xcd >> 1) * 4 + (q16 & 3) : (xcd & 3) * 4 + (idx & 3), nt = MIX ? (xcd & 1) * 4 + (q16 >> 2) : idx >> 2;
  const int g = lane >> 4, q4 = (lane >> 2) & 3, p4 = lane & 3;
  u32x16 sg;
  u32x8 va;
  u32x2 vg;
  {
#define K3_NOB 0
#define K3_NOW 0
    const u32x4 sb = mksrd(ws + WS_A2, K3_NOB ? 0u : 144u * 131072u);
    sg[0] = sb[0]; sg[1] = sb[1]; sg[2] = sb[2]; sg[3] = sb[3];
    sg[4] = sg[5] = sg[6] = sg[7] = 0;
    const u32x4 so = mksrd(Awo, K3_NOW ? 0u : 1024u * 2048u), sd = mksrd(Awd, K3_NOW ? 0u : 56u * 131072u);
    sg[8] = kh ? sd[0] : so[0]; sg[9] = kh ? sd[1] : so[1]; sg[10] = kh ? sd[2] : so[2]; sg[11] = sd[0]; sg[12] = sd[1]; sg[13] = sd[2];
    sg[14] = rfl(kh ? 0 : 3); sg[15] = rfl(lb + kg * 8192 + w * 1024);
    const int x4 = 2 * (g & 1) + (q4 >> 1);
#pragma unroll
    for (int c = 0; c < 4; ++c) { va[c] = lb + kg * 8192 + (4 * g + q4) * 128 + ((c ^ x4) * 32) + 8 * p4; va[4 + c] = va[c] + 3 * 16384; }
    const int row = 8 * w + (lane >> 3);
    vg[0] = (32 * kg + row) * 2048 + nt * 256 + ((((lane & 7) >> 1) ^ ((row >> 1) & 3)) * 32) + (lane & 1) * 16;
    vg[1] = kg * 131072 + (rt * 8 + 2 * w) * 1024 + lane * 16;
  }
  const size_t orow = (size_t)(rt * 128 + wid * 16 + (lane >> 4)) * D_ + nt * 128 + (lane & 15) * 8;
  u32x4 xr[4];
  if (kh) {
#pragma unroll
    for (int i = 0; i < 4; ++i) xr[i] = *(const u32x4*)(Ax + orow + (size_t)4 * i * D_);
  }
  const int pair = MIX ? xcd * 16 + q16 : (xcd & 3) * 32 + idx;
  GAS unsigned* flag = (GAS unsigned*)(ws + WS_FLAGS) + FL_EX + 32 * pair;
  unsigned seen;
  GAS unsigned* const fl = (GAS unsigned*)(ws + WS_FLAGS);
  GAS unsigned* hcp = fl + (kh ? FL_HY : FL_HX) + 32 * (rt >> 1);
  GAS unsigned* ocp = fl + (kh ? FL_HX : FL_O) + 32 * (rt >> 1);
  GAS unsigned* osig = fl + (kh ? FL_HX : FL_O) + 32 * (idx & 7);
  constexpr unsigned H0 = 30 - 16;
  u32x8 st8 = {(unsigned)rfl(kh ? 80 * 131072 : 32 * 131072), (unsigned)rfl(kh ? 24 * 131072 : 0), (unsigned)rfl(kh ? (32 + 2 * H0) * 131072 : 0), (unsigned)rfl(kh ? H0 * 131072 : 0),
               (unsigned)rfl(kh ? 0 : 3 + 1), (unsigned)rfl(kh ? 6 : 4), (unsigned)rfl(kh ? 2 : 0), (unsigned)rfl(kh ? 2 + 1 : 0)};
  f32x16 c0, c1, c2, c3;
  asm volatile(
    "s_nop 4\n\t"
    "s_mov_b32 s43, 0x20000\n\t"
    "s_mov_b32 s53, s73\n\t"
    "s_mov_b32 s54, s72\n\t"
    "s_mov_b32 s55, s77\n\t"
    "s_mov_b32 s40, s47\n\t"
    "s_mov_b32 s41, s48\n\t"
    "s_mov_b32 s42, s49\n\t"
    "s_barrier\n\t"
    "s_add_u32 m0, s51, 0\n\t"
    "s_nop 0\n\t"
    "buffer_load_dwordx4 v56, s[40:43], s53 offen offset:0 lds\n\t"
    "v_mov_b32 v192, 0\n\t"
    "v_mov_b32 v193, 0\n\t"
    "v_mov_b32 v194, 0\n\t"
    "v_mov_b32 v195, 0\n\t"
    "v_mov_b32 v196, 0\n\t"
    "v_mov_b32 v197, 0\n\t"
    "v_mov_b32 v198, 0\n\t"
    "v_mov_b32 v199, 0\n\t"
    "v_mov_b32 v200, 0\n\t"
    "v_mov_b32 v201, 0\n\t"
    "v_mov_b32 v202, 0\n\t"
    "v_mov_b32 v203, 0\n\t"
    "s_add_u32 m0, s51, 3968\n\t"
    "s_nop 0\n\t"
    "buffer_load_dwordx4 v56, s[40:43], s53 offen offset:128 lds\n\t"
    "v_mov_b32 v204, 0\n\t"
    "v_mov_b32 v205, 0\n\t"
    "v_mov_b32 v206, 0\n\t"
    "v_mov_b32 v207, 0\n\t"
    "v_mov_b32 v208, 0\n\t"
    "v_mov_b32 v209, 0\n\t"
    "v_mov_b32 v210, 0\n\t"
    "v_mov_b32 v211, 0\n\t"
    "v_mov_b32 v212, 0\n\t"
    "v_mov_b32 v213, 0\n\t"
    "v_mov_b32 v214, 0\n\t"
    "v_mov_b32 v215, 0\n\t"
    "v_mov_b32 v216, 0\n\t"
    "v_mov_b32 v217, 0\n\t"
    "v_mov_b32 v218, 0\n\t"
    "v_mov_b32 v219, 0\n\t"
    "v_mov_b32 v220, 0\n\t"
    "v_mov_b32 v221, 0\n\t"
    "v_mov_b32 v222, 0\n\t"
    "v_mov_b32 v223, 0\n\t"
    "v_mov_b32 v224, 0\n\t"
    "v_mov_b32 v225, 0\n\t"
    "v_mov_b32 v226, 0\n\t"
    "v_mov_b32 v227, 0\n\t"
    "v_mov_b32 v228, 0\n\t"
    "v_mov_b32 v229, 0\n\t"
    "v_mov_b32 v230, 0\n\t"
    "v_mov_b32 v231, 0\n\t"
    "v_mov_b32 v232, 0\n\t"
    "v_mov_b32 v233, 0\n\t"
    "v_mov_b32 v234, 0\n\t"
    "v_mov_b32 v235, 0\n\t"
    "v_mov_b32 v236, 0\n\t"
    "v_mov_b32 v237, 0\n\t"
    "v_mov_b32 v238, 0\n\t"
    "v_mov_b32 v239, 0\n\t"
    "s_mov_b32 s62, 0x40000\n\t"
    "LK3HC_%=:\n\t"
    "v_readfirstlane_b32 s63, v59\n\t"
    "s_nop 3\n\t"
    "s_cmp_ge_u32 s63, 16\n\t"
    "s_cbranch_scc1 LK3HO_%=\n\t"
    "global_load_dword v59, v[66:67], off sc1\n\t"
    "s_waitcnt vmcnt(0)\n\t"
    "s_sub_u32 s62, s62, 1\n\t"
    "s_cmp_lg_u32 s62, 0\n\t"
    "s_cbranch_scc1 LK3HC_%=\n\t"
    "LK3HO_%=:\n\t"
    "v_mov_b32 v59, 0\n\t"
    "buffer_load_dwordx4 v[112:115], v57, s[36:39], s54 offen offset:0 sc1\n\t"
    "buffer_load_dwordx4 v[116:119], v57, s[36:39], s54 offen offset:1024 sc1\n\t"
    "s_add_u32 s53, s53, 131072\n\t"
    "s_add_u32 s54, s54, 262144\n\t"
    "s_add_u32 m0, s51, 16384\n\t"
    "s_nop 0\n\t"
    "buffer_load_dwordx4 v56, s[40:43], s53 offen offset:0 lds\n\t"
    "v_mov_b32 v240, 0\n\t"
    "v_mov_b32 v241, 0\n\t"
    "v_mov_b32 v242, 0\n\t"
    "v_mov_b32 v243, 0\n\t"
    "s_add_u32 m0, s51, 20352\n\t"
    "s_nop 0\n\t"
    "buffer_load_dwordx4 v56, s[40:43], s53 offen offset:128 lds\n\t"
    "v_mov_b32 v244, 0\n\t"
    "v_mov_b32 v245, 0\n\t"
    "v_mov_b32 v246, 0\n\t"
    "v_mov_b32 v247, 0\n\t"
    "buffer_load_dwordx4 v[120:123], v57, s[36:39], s54 offen offset:0 sc1\n\t"
    "buffer_load_dwordx4 v[124:127], v57, s[36:39], s54 offen offset:1024 sc1\n\t"
    "s_add_u32 s53, s53, 131072\n\t"
    "s_add_u32 s54, s54, 262144\n\t"
    "s_add_u32 m0, s51, 32768\n\t"
    "s_nop 0\n\t"
    "buffer_load_dwordx4 v56, s[40:43], s53 offen offset:0 lds\n\t"
    "v_mov_b32 v248, 0\n\t"
    "v_mov_b32 v249, 0\n\t"
    "v_mov_b32 v250, 0\n\t"
    "v_mov_b32 v251, 0\n\t"
    "s_add_u32 m0, s51, 36736\n\t"
    "s_nop 0\n\t"
    "buffer_load_dwordx4 v56, s[40:43], s53 offen offset:128 lds\n\t"
    "v_mov_b32 v252, 0\n\t"
    "v_mov_b32 v253, 0\n\t"
    "v_mov_b32 v254, 0\n\t"
    "v_mov_b32 v255, 0\n\t"
    "buffer_load_dwordx4 v[128:131], v57, s[36:39], s54 offen offset:0 sc1\n\t"
    "buffer_load_dwordx4 v[132:135], v57, s[36:39], s54 offen offset:1024 sc1\n\t"
    "s_add_u32 s53, s53, 131072\n\t"
    "s_add_u32 s54, s54, 262144\n\t"
    "s_waitcnt vmcnt(8)\n\t"
    "s_barrier\n\t"
    "s_cmp_lg_u32 s64, 0\n\t"
    "s_cbranch_scc1 LK3NS_%=\n\t"
    "s_mov_b64 exec, 1\n\t"
    "v_mov_b32 v59, 1\n\t"
    "global_atomic_add v[64:65], v59, off sc1\n\t"
    "s_mov_b64 exec, -1\n\t"
    "LK3NS_%=:\n\t"
    "ds_read_b64_tr_b16 v[160:161], v48 offset:0\n\t"
    "ds_read_b64_tr_b16 v[162:163], v48 offset:2048\n\t"
    "ds_read_b64_tr_b16 v[164:165], v49 offset:0\n\t"
    "ds_read_b64_tr_b16 v[166:167], v49 offset:2048\n\t"
    "ds_read_b64_tr_b16 v[168:169], v50 offset:0\n\t"
    "ds_read_b64_tr_b16 v[170:171], v50 offset:2048\n\t"
    "ds_read_b64_tr_b16 v[172:173], v51 offset:0\n\t"
    "ds_read_b64_tr_b16 v[174:175], v51 offset:2048\n\t"
    "ds_read_b64_tr_b16 v[176:177], v48 offset:4096\n\t"
    "ds_read_b64_tr_b16 v[178:179], v48 offset:6144\n\t"
    "ds_read_b64_tr_b16 v[180:181], v49 offset:4096\n\t"
    "ds_read_b64_tr_b16 v[182:183], v49 offset:6144\n\t"
    "ds_read_b64_tr_b16 v[184:185], v50 offset:4096\n\t"
    "ds_read_b64_tr_b16 v[186:187], v50 offset:6144\n\t"
    "ds_read_b64_tr_b16 v[188:189], v51 offset:4096\n\t"
    "ds_read_b64_tr_b16 v[190:191], v51 offset:6144\n\t"
    "s_waitcnt vmcnt(4)\n\t"
    "s_barrier\n\t"
    "s_waitcnt lgkmcnt(8)\n\t"
    "v_mfma_f32_16x16x32_bf16 v[192:195], v[160:163], v[112:115], v[192:195]\n\t"
    "s_add_u32 m0, s51, 49152\n\t"
    "buffer_load_dwordx4 v[136:139], v57, s[36:39], s54 offen offset:0 sc1\n\t"
    "v_mfma_f32_16x16x32_bf16 v[196:199], v[160:163], v[116:119], v[196:199]\n\t"
    "ds_read_b64_tr_b16 v[160:161], v48 offset:16384\n\t"
    "ds_read_b64_tr_b16 v[162:163], v48 offset:18432\n\t"
    "v_mfma_f32_16x16x32_bf16 v[200:203], v[164:167], v[112:115], v[200:203]\n\t"
    "buffer_load_dwordx4 v56, s[40:43], s53 offen offset:0 lds\n\t"
    "s_add_u32 m0, s51, 53120\n\t"
    "v_mfma_f32_16x16x32_bf16 v[204:207], v[164:167], v[116:119], v[204:207]\n\t"
    "ds_read_b64_tr_b16 v[164:165], v49 offset:16384\n\t"
    "ds_read_b64_tr_b16 v[166:167], v49 offset:18432\n\t"
    "v_mfma_f32_16x16x32_bf16 v[208:211], v[168:171], v[112:115], v[208:211]\n\t"
    "buffer_load_dwordx4 v[140:143], v57, s[36:39], s54 offen offset:1024 sc1\n\t"
    "buffer_load_dwordx4 v56, s[40:43], s53 offen offset:128 lds\n\t"
    "v_mfma_f32_16x16x32_bf16 v[212:215], v[168:171], v[116:119], v[212:215]\n\t"
    "ds_read_b64_tr_b16 v[168:169], v50 offset:16384\n\t"
    "ds_read_b64_tr_b16 v[170:171], v50 offset:18432\n\t"
    "v_mfma_f32_16x16x32_bf16 v[216:219], v[172:175], v[112:115], v[216:219]\n\t"
    "s_add_u32 s53, s53, 131072\n\t"
    "s_add_u32 s54, s54, 262144\n\t"
    "v_mfma_f32_16x16x32_bf16 v[220:223], v[172:175], v[116:119], v[220:223]\n\t"
    "ds_read_b64_tr_b16 v[172:173], v51 offset:16384\n\t"
    "ds_read_b64_tr_b16 v[174:175], v51 offset:18432\n\t"
    "s_waitcnt lgkmcnt(8)\n\t"
    "v_mfma_f32_16x16x32_bf16 v[224:227], v[176:179], v[112:115], v[224:227]\n\t"
    "s_add_u32 m0, s51, 65536\n\t"
    "buffer_load_dwordx4 v[144:147], v57, s[36:39], s54 offen offset:0 sc1\n\t"
    "v_mfma_f32_16x16x32_bf16 v[228:231], v[176:179], v[116:119], v[228:231]\n\t"
    "ds_read_b64_tr_b16 v[176:177], v48 offset:20480\n\t"
    "ds_read_b64_tr_b16 v[178:179], v48 offset:22528\n\t"
    "v_mfma_f32_16x16x32_bf16 v[232:235], v[180:183], v[112:115], v[232:235]\n\t"
    "buffer_load_dwordx4 v56, s[40:43], s53 offen offset:0 lds\n\t"
    "s_add_u32 m0, s51, 69504\n\t"
    "v_mfma_f32_16x16x32_bf16 v[236:239], v[180:183], v[116:119], v[236:239]\n\t"
    "ds_read_b64_tr_b16 v[180:181], v49 offset:20480\n\t"
    "ds_read_b64_tr_b16 v[182:183], v49 offset:22528\n\t"
    "v_mfma_f32_16x16x32_bf16 v[240:243], v[184:187], v[112:115], v[240:243]\n\t"
    "buffer_load_dwordx4 v[148:151], v57, s[36:39], s54 offen offset:1024 sc1\n\t"
    "buffer_load_dwordx4 v56, s[40:43], s53 offen offset:128 lds\n\t"
    "v_mfma_f32_16x16x32_bf16 v[244:247], v[184:187], v[116:119], v[244:247]\n\t"
    "ds_read_b64_tr_b16 v[184:185], v50 offset:20480\n\t"
    "ds_read_b64_tr_b16 v[186:187], v50 offset:22528\n\t"
    "v_mfma_f32_16x16x32_bf16 v[248:251], v[188:191], v[112:115], v[248:251]\n\t"
    "s_add_u32 s53, s53, 131072\n\t"
    "s_add_u32 s54, s54, 262144\n\t"
    "v_mfma_f32_16x16x32_bf16 v[252:255], v[188:191], v[116:119], v[252:255]\n\t"
    "ds_read_b64_tr_b16 v[188:189], v51 offset:20480\n\t"
    "ds_read_b64_tr_b16 v[190:191], v51 offset:22528\n\t"
    "s_waitcnt vmcnt(8)\n\t"
    "s_barrier\n\t"
    "s_waitcnt lgkmcnt(8)\n\t"
    "v_mfma_f32_16x16x32_bf16 v[192:195], v[160:163], v[120:123], v[192:195]\n\t"
    "s_add_u32 m0, s51, 81920\n\t"
    "buffer_load_dwordx4 v[152:155], v57, s[36:39], s54 offen offset:0 sc1\n\t"
    "v_mfma_f32_16x16x32_bf16 v[196:199], v[160:163], v[124:127], v[196:199]\n\t"
    "ds_read_b64_tr_b16 v[160:161], v48 offset:32768\n\t"
    "ds_read_b64_tr_b16 v[162:163], v48 offset:34816\n\t"
    "v_mfma_f32_16x16x32_bf16 v[200:203], v[164:167], v[120:123], v[200:203]\n\t"
    "buffer_load_dwordx4 v56, s[40:43], s53 offen offset:0 lds\n\t"
    "s_add_u32 m0, s51, 85888\n\t"
    "v_mfma_f32_16x16x32_bf16 v[204:207], v[164:167], v[124:127], v[204:207]\n\t"
    "ds_read_b64_tr_b16 v[164:165], v49 offset:32768\n\t"
    "ds_read_b64_tr_b16 v[166:167], v49 offset:34816\n\t"
    "v_mfma_f32_16x16x32_bf16 v[208:211], v[168:171], v[120:123], v[208:211]\n\t"
    "buffer_load_dwordx4 v[156:159], v57, s[36:39], s54 offen offset:1024 sc1\n\t"
    "buffer_load_dwordx4 v56, s[40:43], s53 offen offset:128 lds\n\t"
    "v_mfma_f32_16x16x32_bf16 v[212:215], v[168:171], v[124:127], v[212:215]\n\t"
    "ds_read_b64_tr_b16 v[168:169], v50 offset:32768\n\t"
    "ds_read_b64_tr_b16 v[170:171], v50 offset:34816\n\t"
    "v_mfma_f32_16x16x32_bf16 v[216:219], v[172:175], v[120:123], v[216:219]\n\t"
    "s_add_u32 s53, s53, 131072\n\t"
    "s_add_u32 s54, s54, 262144\n\t"
    "v_mfma_f32_16x16x32_bf16 v[220:223], v[172:175], v[124:127], v[220:223]\n\t"
    "ds_read_b64_tr_b16 v[172:173], v51 offset:32768\n\t"
    "ds_read_b64_tr_b16 v[174:175], v51 offset:34816\n\t"
    "s_waitcnt lgkmcnt(8)\n\t"
    "v_mfma_f32_16x16x32_bf16 v[224:227], v[176:179], v[120:123], v[224:227]\n\t"
    "s_add_u32 m0, s51, 0\n\t"
    "buffer_load_dwordx4 v[112:115], v57, s[36:39], s54 offen offset:0 sc1\n\t"
    "v_mfma_f32_16x16x32_bf16 v[228:231], v[176:179], v[124:127], v[228:231]\n\t"
    "ds_read_b64_tr_b16 v[176:177], v48 offset:36864\n\t"
    "ds_read_b64_tr_b16 v[178:179], v48 offset:38912\n\t"
    "v_mfma_f32_16x16x32_bf16 v[232:235], v[180:183], v[120:123], v[232:235]\n\t"
    "buffer_load_dwordx4 v56, s[40:43], s53 offen offset:0 lds\n\t"
    "s_add_u32 m0, s51, 3968\n\t"
    "v_mfma_f32_16x16x32_bf16 v[236:239], v[180:183], v[124:127], v[236:239]\n\t"
    "ds_read_b64_tr_b16 v[180:181], v49 offset:36864\n\t"
    "ds_read_b64_tr_b16 v[182:183], v49 offset:38912\n\t"
    "v_mfma_f32_16x16x32_bf16 v[240:243], v[184:187], v[120:123], v[240:243]\n\t"
    "buffer_load_dwordx4 v[116:119], v57, s[36:39], s54 offen offset:1024 sc1\n\t"
    "buffer_load_dwordx4 v56, s[40:43], s53 offen offset:128 lds\n\t"
    "v_mfma_f32_16x16x32_bf16 v[244:247], v[184:187], v[124:127], v[244:247]\n\t"
    "ds_read_b64_tr_b16 v[184:185], v50 offset:36864\n\t"
    "ds_read_b64_tr_b16 v[186:187], v50 offset:38912\n\t"
    "v_mfma_f32_16x16x32_bf16 v[248:251], v[188:191], v[120:123], v[248:251]\n\t"
    "s_add_u32 s53, s53, 131072\n\t"
    "s_add_u32 s54, s54, 262144\n\t"
    "v_mfma_f32_16x16x32_bf16 v[252:255], v[188:191], v[124:127], v[252:255]\n\t"
    "ds_read_b64_tr_b16 v[188:189], v51 offset:36864\n\t"
    "ds_read_b64_tr_b16 v[190:191], v51 offset:38912\n\t"
    "LK3_%=:\n\t"
    "s_waitcnt vmcnt(12)\n\t"
    "s_barrier\n\t"
    "s_waitcnt lgkmcnt(8)\n\t"
    "v_mfma_f32_16x16x32_bf16 v[192:195], v[160:163], v[128:131], v[192:195]\n\t"
    "s_add_u32 m0, s51, 16384\n\t"
    "v_mfma_f32_16x16x32_bf16 v[196:199], v[160:163], v[132:135], v[196:199]\n\t"
    "ds_read_b64_tr_b16 v[160:161], v52 offset:0\n\t"
    "ds_read_b64_tr_b16 v[162:163], v52 offset:2048\n\t"
    "v_mfma_f32_16x16x32_bf16 v[200:203], v[164:167], v[128:131], v[200:203]\n\t"
    "buffer_load_dwordx4 v56, s[40:43], s53 offen offset:0 lds\n\t"
    "v_mfma_f32_16x16x32_bf16 v[204:207], v[164:167], v[132:135], v[204:207]\n\t"
    "ds_read_b64_tr_b16 v[164:165], v53 offset:0\n\t"
    "ds_read_b64_tr_b16 v[166:167], v53 offset:2048\n\t"
    "v_mfma_f32_16x16x32_bf16 v[208:211], v[168:171], v[128:131], v[208:211]\n\t"
    "s_add_u32 m0, s51, 20352\n\t"
    "v_mfma_f32_16x16x32_bf16 v[212:215], v[168:171], v[132:135], v[212:215]\n\t"
    "ds_read_b64_tr_b16 v[168:169], v54 offset:0\n\t"
    "ds_read_b64_tr_b16 v[170:171], v54 offset:2048\n\t"
    "v_mfma_f32_16x16x32_bf16 v[216:219], v[172:175], v[128:131], v[216:219]\n\t"
    "buffer_load_dwordx4 v56, s[40:43], s53 offen offset:128 lds\n\t"
    "v_mfma_f32_16x16x32_bf16 v[220:223], v[172:175], v[132:135], v[220:223]\n\t"
    "ds_read_b64_tr_b16 v[172:173], v55 offset:0\n\t"
    "ds_read_b64_tr_b16 v[174:175], v55 offset:2048\n\t"
    "s_waitcnt lgkmcnt(8)\n\t"
    "v_mfma_f32_16x16x32_bf16 v[224:227], v[176:179], v[128:131], v[224:227]\n\t"
    "buffer_load_dwordx4 v[120:123], v57, s[36:39], s54 offen offset:0 sc1\n\t"
    "v_mfma_f32_16x16x32_bf16 v[228:231], v[176:179], v[132:135], v[228:231]\n\t"
    "ds_read_b64_tr_b16 v[176:177], v52 offset:4096\n\t"
    "ds_read_b64_tr_b16 v[178:179], v52 offset:6144\n\t"
    "v_mfma_f32_16x16x32_bf16 v[232:235], v[180:183], v[128:131], v[232:235]\n\t"
    "buffer_load_dwordx4 v[124:127], v57, s[36:39], s54 offen offset:1024 sc1\n\t"
    "v_mfma_f32_16x16x32_bf16 v[236:239], v[180:183], v[132:135], v[236:239]\n\t"
    "ds_read_b64_tr_b16 v[180:181], v53 offset:4096\n\t"
    "ds_read_b64_tr_b16 v[182:183], v53 offset:6144\n\t"
    "v_mfma_f32_16x16x32_bf16 v[240:243], v[184:187], v[128:131], v[240:243]\n\t"
    "s_add_u32 s53, s53, 131072\n\t"
    "v_mfma_f32_16x16x32_bf16 v[244:247], v[184:187], v[132:135], v[244:247]\n\t"
    "ds_read_b64_tr_b16 v[184:185], v54 offset:4096\n\t"
    "ds_read_b64_tr_b16 v[186:187], v54 offset:6144\n\t"
    "v_mfma_f32_16x16x32_bf16 v[248:251], v[188:191], v[128:131], v[248:251]\n\t"
    "s_add_u32 s54, s54, 262144\n\t"
    "v_mfma_f32_16x16x32_bf16 v[252:255], v[188:191], v[132:135], v[252:255]\n\t"
    "ds_read_b64_tr_b16 v[188:189], v55 offset:4096\n\t"
    "ds_read_b64_tr_b16 v[190:191], v55 offset:6144\n\t"
    "s_waitcnt vmcnt(12)\n\t"
    "s_barrier\n\t"
    "s_cmp_eq_u32 s55, s76\n\t"
    "s_cbranch_scc1 LK3PLA_%=\n\t"
    "LK3PBA_%=:\n\t"
    "s_cmp_eq_u32 s55, s50\n\t"
    "s_cbranch_scc1 LK3SWA_%=\n\t"
    "LK3SBA_%=:\n\t"
    "s_cmp_eq_u32 s55, s79\n\t"
    "s_cbranch_scc1 LK3PLB_%=\n\t"
    "LK3PBB_%=:\n\t"
    "s_cmp_eq_u32 s55, s78\n\t"
    "s_cbranch_scc1 LK3SWB_%=\n\t"
    "LK3SBB_%=:\n\t"
    "s_waitcnt lgkmcnt(8)\n\t"
    "v_mfma_f32_16x16x32_bf16 v[192:195], v[160:163], v[136:139], v[192:195]\n\t"
    "s_add_u32 m0, s51, 32768\n\t"
    "v_mfma_f32_16x16x32_bf16 v[196:199], v[160:163], v[140:143], v[196:199]\n\t"
    "ds_read_b64_tr_b16 v[160:161], v52 offset:16384\n\t"
    "ds_read_b64_tr_b16 v[162:163], v52 offset:18432\n\t"
    "v_mfma_f32_16x16x32_bf16 v[200:203], v[164:167], v[136:139], v[200:203]\n\t"
    "buffer_load_dwordx4 v56, s[40:43], s53 offen offset:0 lds\n\t"
    "v_mfma_f32_16x16x32_bf16 v[204:207], v[164:167], v[140:143], v[204:207]\n\t"
    "ds_read_b64_tr_b16 v[164:165], v53 offset:16384\n\t"
    "ds_read_b64_tr_b16 v[166:167], v53 offset:18432\n\t"
    "v_mfma_f32_16x16x32_bf16 v[208:211], v[168:171], v[136:139], v[208:211]\n\t"
    "s_add_u32 m0, s51, 36736\n\t"
    "v_mfma_f32_16x16x32_bf16 v[212:215], v[168:171], v[140:143], v[212:215]\n\t"
    "ds_read_b64_tr_b16 v[168:169], v54 offset:16384\n\t"
    "ds_read_b64_tr_b16 v[170:171], v54 offset:18432\n\t"
    "v_mfma_f32_16x16x32_bf16 v[216:219], v[172:175], v[136:139], v[216:219]\n\t"
    "buffer_load_dwordx4 v56, s[40:43], s53 offen offset:128 lds\n\t"
    "v_mfma_f32_16x16x32_bf16 v[220:223], v[172:175], v[140:143], v[220:223]\n\t"
    "ds_read_b64_tr_b16 v[172:173], v55 offset:16384\n\t"
    "ds_read_b64_tr_b16 v[174:175], v55 offset:18432\n\t"
    "s_waitcnt lgkmcnt(8)\n\t"
    "v_mfma_f32_16x16x32_bf16 v[224:227], v[176:179], v[136:139], v[224:227]\n\t"
    "buffer_load_dwordx4 v[128:131], v57, s[36:39], s54 offen offset:0 sc1\n\t"
    "v_mfma_f32_16x16x32_bf16 v[228:231], v[176:179], v[140:143], v[228:231]\n\t"
    "ds_read_b64_tr_b16 v[176:177], v52 offset:20480\n\t"
    "ds_read_b64_tr_b16 v[178:179], v52 offset:22528\n\t"
    "v_mfma_f32_16x16x32_bf16 v[232:235], v[180:183], v[136:139], v[232:235]\n\t"
    "buffer_load_dwordx4 v[132:135], v57, s[36:39], s54 offen offset:1024 sc1\n\t"
    "v_mfma_f32_16x16x32_bf16 v[236:239], v[180:183], v[140:143], v[236:239]\n\t"
    "ds_read_b64_tr_b16 v[180:181], v53 offset:20480\n\t"
    "ds_read_b64_tr_b16 v[182:183], v53 offset:22528\n\t"
    "v_mfma_f32_16x16x32_bf16 v[240:243], v[184:187], v[136:139], v[240:243]\n\t"
    "s_add_u32 s53, s53, 131072\n\t"
    "v_mfma_f32_16x16x32_bf16 v[244:247], v[184:187], v[140:143], v[244:247]\n\t"
    "ds_read_b64_tr_b16 v[184:185], v54 offset:20480\n\t"
    "ds_read_b64_tr_b16 v[186:187], v54 offset:22528\n\t"
    "v_mfma_f32_16x16x32_bf16 v[248:251], v[188:191], v[136:139], v[248:251]\n\t"
    "s_add_u32 s54, s54, 262144\n\t"
    "v_mfma_f32_16x16x32_bf16 v[252:255], v[188:191], v[140:143], v[252:255]\n\t"
    "ds_read_b64_tr_b16 v[188:189], v55 offset:20480\n\t"
    "ds_read_b64_tr_b16 v[190:191], v55 offset:22528\n\t"
    "s_waitcnt vmcnt(12)\n\t"
    "s_barrier\n\t"
    "s_waitcnt lgkmcnt(8)\n\t"
    "v_mfma_f32_16x16x32_bf16 v[192:195], v[160:163], v[144:147], v[192:195]\n\t"
    "s_add_u32 m0, s51, 49152\n\t"
    "v_mfma_f32_16x16x32_bf16 v[196:199], v[160:163], v[148:151], v[196:199]\n\t"
    "ds_read_b64_tr_b16 v[160:161], v52 offset:32768\n\t"
    "ds_read_b64_tr_b16 v[162:163], v52 offset:34816\n\t"
    "v_mfma_f32_16x16x32_bf16 v[200:203], v[164:167], v[144:147], v[200:203]\n\t"
    "buffer_load_dwordx4 v56, s[40:43], s53 offen offset:0 lds\n\t"
    "v_mfma_f32_16x16x32_bf16 v[204:207], v[164:167], v[148:151], v[204:207]\n\t"
    "ds_read_b64_tr_b16 v[164:165], v53 offset:32768\n\t"
    "ds_read_b64_tr_b16 v[166:167], v53 offset:34816\n\t"
    "v_mfma_f32_16x16x32_bf16 v[208:211], v[168:171], v[144:147], v[208:211]\n\t"
    "s_add_u32 m0, s51, 53120\n\t"
    "v_mfma_f32_16x16x32_bf16 v[212:215], v[168:171], v[148:151], v[212:215]\n\t"
    "ds_read_b64_tr_b16 v[168:169], v54 offset:32768\n\t"
    "ds_read_b64_tr_b16 v[170:171], v54 offset:34816\n\t"
    "v_mfma_f32_16x16x32_bf16 v[216:219], v[172:175], v[144:147], v[216:219]\n\t"
    "buffer_load_dwordx4 v56, s[40:43], s53 offen offset:128 lds\n\t"
    "v_mfma_f32_16x16x32_bf16 v[220:223], v[172:175], v[148:151], v[220:223]\n\t"
    "ds_read_b64_tr_b16 v[172:173], v55 offset:32768\n\t"
    "ds_read_b64_tr_b16 v[174:175], v55 offset:34816\n\t"
    "s_waitcnt lgkmcnt(8)\n\t"
    "v_mfma_f32_16x16x32_bf16 v[224:227], v[176:179], v[144:147], v[224:227]\n\t"
    "buffer_load_dwordx4 v[136:139], v57, s[36:39], s54 offen offset:0 sc1\n\t"
    "v_mfma_f32_16x16x32_bf16 v[228:231], v[176:179], v[148:151], v[228:231]\n\t"
    "ds_read_b64_tr_b16 v[176:177], v52 offset:36864\n\t"
    "ds_read_b64_tr_b16 v[178:179], v52 offset:38912\n\t"
    "v_mfma_f32_16x16x32_bf16 v[232:235], v[180:183], v[144:147], v[232:235]\n\t"
    "buffer_load_dwordx4 v[140:143], v57, s[36:39], s54 offen offset:1024 sc1\n\t"
    "v_mfma_f32_16x16x32_bf16 v[236:239], v[180:183], v[148:151], v[236:239]\n\t"
    "ds_read_b64_tr_b16 v[180:181], v53 offset:36864\n\t"
    "ds_read_b64_tr_b16 v[182:183], v53 offset:38912\n\t"
    "v_mfma_f32_16x16x32_bf16 v[240:243], v[184:187], v[144:147], v[240:243]\n\t"
    "s_add_u32 s53, s53, 131072\n\t"
    "v_mfma_f32_16x16x32_bf16 v[244:247], v[184:187], v[148:151], v[244:247]\n\t"
    "ds_read_b64_tr_b16 v[184:185], v54 offset:36864\n\t"
    "ds_read_b64_tr_b16 v[186:187], v54 offset:38912\n\t"
    "v_mfma_f32_16x16x32_bf16 v[248:251], v[188:191], v[144:147], v[248:251]\n\t"
    "s_add_u32 s54, s54, 262144\n\t"
    "v_mfma_f32_16x16x32_bf16 v[252:255], v[188:191], v[148:151], v[252:255]\n\t"
    "ds_read_b64_tr_b16 v[188:189], v55 offset:36864\n\t"
    "ds_read_b64_tr_b16 v[190:191], v55 offset:38912\n\t"
    "s_waitcnt vmcnt(12)\n\t"
    "s_barrier\n\t"
    "s_waitcnt lgkmcnt(8)\n\t"
    "v_mfma_f32_16x16x32_bf16 v[192:195], v[160:163], v[152:155], v[192:195]\n\t"
    "s_add_u32 m0, s51, 65536\n\t"
    "v_mfma_f32_16x16x32_bf16 v[196:199], v[160:163], v[156:159], v[196:199]\n\t"
    "ds_read_b64_tr_b16 v[160:161], v48 offset:0\n\t"
    "ds_read_b64_tr_b16 v[162:163], v48 offset:2048\n\t"
    "v_mfma_f32_16x16x32_bf16 v[200:203], v[164:167], v[152:155], v[200:203]\n\t"
    "buffer_load_dwordx4 v56, s[40:43], s53 offen offset:0 lds\n\t"
    "v_mfma_f32_16x16x32_bf16 v[204:207], v[164:167], v[156:159], v[204:207]\n\t"
    "ds_read_b64_tr_b16 v[164:165], v49 offset:0\n\t"
    "ds_read_b64_tr_b16 v[166:167], v49 offset:2048\n\t"
    "v_mfma_f32_16x16x32_bf16 v[208:211], v[168:171], v[152:155], v[208:211]\n\t"
    "s_add_u32 m0, s51, 69504\n\t"
    "v_mfma_f32_16x16x32_bf16 v[212:215], v[168:171], v[156:159], v[212:215]\n\t"
    "ds_read_b64_tr_b16 v[168:169], v50 offset:0\n\t"
    "ds_read_b64_tr_b16 v[170:171], v50 offset:2048\n\t"
    "v_mfma_f32_16x16x32_bf16 v[216:219], v[172:175], v[152:155], v[216:219]\n\t"
    "buffer_load_dwordx4 v56, s[40:43], s53 offen offset:128 lds\n\t"
    "v_mfma_f32_16x16x32_bf16 v[220:223], v[172:175], v[156:159], v[220:223]\n\t"
    "ds_read_b64_tr_b16 v[172:173], v51 offset:0\n\t"
    "ds_read_b64_tr_b16 v[174:175], v51 offset:2048\n\t"
    "s_waitcnt lgkmcnt(8)\n\t"
    "v_mfma_f32_16x16x32_bf16 v[224:227], v[176:179], v[152:155], v[224:227]\n\t"
    "buffer_load_dwordx4 v[144:147], v57, s[36:39], s54 offen offset:0 sc1\n\t"
    "v_mfma_f32_16x16x32_bf16 v[228:231], v[176:179], v[156:159], v[228:231]\n\t"
    "ds_read_b64_tr_b16 v[176:177], v48 offset:4096\n\t"
    "ds_read_b64_tr_b16 v[178:179], v48 offset:6144\n\t"
    "v_mfma_f32_16x16x32_bf16 v[232:235], v[180:183], v[152:155], v[232:235]\n\t"
    "buffer_load_dwordx4 v[148:151], v57, s[36:39], s54 offen offset:1024 sc1\n\t"
    "v_mfma_f32_16x16x32_bf16 v[236:239], v[180:183], v[156:159], v[236:239]\n\t"
    "ds_read_b64_tr_b16 v[180:181], v49 offset:4096\n\t"
    "ds_read_b64_tr_b16 v[182:183], v49 offset:6144\n\t"
    "v_mfma_f32_16x16x32_bf16 v[240:243], v[184:187], v[152:155], v[240:243]\n\t"
    "s_add_u32 s53, s53, 131072\n\t"
    "v_mfma_f32_16x16x32_bf16 v[244:247], v[184:187], v[156:159], v[244:247]\n\t"
    "ds_read_b64_tr_b16 v[184:185], v50 offset:4096\n\t"
    "ds_read_b64_tr_b16 v[186:187], v50 offset:6144\n\t"
    "v_mfma_f32_16x16x32_bf16 v[248:251], v[188:191], v[152:155], v[248:251]\n\t"
    "s_add_u32 s54, s54, 262144\n\t"
    "v_mfma_f32_16x16x32_bf16 v[252:255], v[188:191], v[156:159], v[252:255]\n\t"
    "ds_read_b64_tr_b16 v[188:189], v51 offset:4096\n\t"
    "ds_read_b64_tr_b16 v[190:191], v51 offset:6144\n\t"
    "s_waitcnt vmcnt(12)\n\t"
    "s_barrier\n\t"
    "s_waitcnt lgkmcnt(8)\n\t"
    "v_mfma_f32_16x16x32_bf16 v[192:195], v[160:163], v[112:115], v[192:195]\n\t"
    "s_add_u32 m0, s51, 81920\n\t"
    "v_mfma_f32_16x16x32_bf16 v[196:199], v[160:163], v[116:119], v[196:199]\n\t"
    "ds_read_b64_tr_b16 v[160:161], v48 offset:16384\n\t"
    "ds_read_b64_tr_b16 v[162:163], v48 offset:18432\n\t"
    "v_mfma_f32_16x16x32_bf16 v[200:203], v[164:167], v[112:115], v[200:203]\n\t"
    "buffer_load_dwordx4 v56, s[40:43], s53 offen offset:0 lds\n\t"
    "v_mfma_f32_16x16x32_bf16 v[204:207], v[164:167], v[116:119], v[204:207]\n\t"
    "ds_read_b64_tr_b16 v[164:165], v49 offset:16384\n\t"
    "ds_read_b64_tr_b16 v[166:167], v49 offset:18432\n\t"
    "v_mfma_f32_16x16x32_bf16 v[208:211], v[168:171], v[112:115], v[208:211]\n\t"
    "s_add_u32 m0, s51, 85888\n\t"
    "v_mfma_f32_16x16x32_bf16 v[212:215], v[168:171], v[116:119], v[212:215]\n\t"
    "ds_read_b64_tr_b16 v[168:169], v50 offset:16384\n\t"
    "ds_read_b64_tr_b16 v[170:171], v50 offset:18432\n\t"
    "v_mfma_f32_16x16x32_bf16 v[216:219], v[172:175], v[112:115], v[216:219]\n\t"
    "buffer_load_dwordx4 v56, s[40:43], s53 offen offset:128 lds\n\t"
    "v_mfma_f32_16x16x32_bf16 v[220:223], v[172:175], v[116:119], v[220:223]\n\t"
    "ds_read_b64_tr_b16 v[172:173], v51 offset:16384\n\t"
    "ds_read_b64_tr_b16 v[174:175], v51 offset:18432\n\t"
    "s_waitcnt lgkmcnt(8)\n\t"
    "v_mfma_f32_16x16x32_bf16 v[224:227], v[176:179], v[112:115], v[224:227]\n\t"
    "buffer_load_dwordx4 v[152:155], v57, s[36:39], s54 offen offset:0 sc1\n\t"
    "v_mfma_f32_16x16x32_bf16 v[228:231], v[176:179], v[116:119], v[228:231]\n\t"
    "ds_read_b64_tr_b16 v[176:177], v48 offset:20480\n\t"
    "ds_read_b64_tr_b16 v[178:179], v48 offset:22528\n\t"
    "v_mfma_f32_16x16x32_bf16 v[232:235], v[180:183], v[112:115], v[232:235]\n\t"
    "buffer_load_dwordx4 v[156:159], v57, s[36:39], s54 offen offset:1024 sc1\n\t"
    "v_mfma_f32_16x16x32_bf16 v[236:239], v[180:183], v[116:119], v[236:239]\n\t"
    "ds_read_b64_tr_b16 v[180:181], v49 offset:20480\n\t"
    "ds_read_b64_tr_b16 v[182:183], v49 offset:22528\n\t"
    "v_mfma_f32_16x16x32_bf16 v[240:243], v[184:187], v[112:115], v[240:243]\n\t"
    "s_add_u32 s53, s53, 131072\n\t"
    "v_mfma_f32_16x16x32_bf16 v[244:247], v[184:187], v[116:119], v[244:247]\n\t"
    "ds_read_b64_tr_b16 v[184:185], v50 offset:20480\n\t"
    "ds_read_b64_tr_b16 v[186:187], v50 offset:22528\n\t"
    "v_mfma_f32_16x16x32_bf16 v[248:251], v[188:191], v[112:115], v[248:251]\n\t"
    "s_add_u32 s54, s54, 262144\n\t"
    "v_mfma_f32_16x16x32_bf16 v[252:255], v[188:191], v[116:119], v[252:255]\n\t"
    "ds_read_b64_tr_b16 v[188:189], v51 offset:20480\n\t"
    "ds_read_b64_tr_b16 v[190:191], v51 offset:22528\n\t"
    "s_waitcnt vmcnt(12)\n\t"
    "s_barrier\n\t"
    "s_waitcnt lgkmcnt(8)\n\t"
    "v_mfma_f32_16x16x32_bf16 v[192:195], v[160:163], v[120:123], v[192:195]\n\t"
    "s_add_u32 m0, s51, 0\n\t"
    "v_mfma_f32_16x16x32_bf16 v[196:199], v[160:163], v[124:127], v[196:199]\n\t"
    "ds_read_b64_tr_b16 v[160:161], v48 offset:32768\n\t"
    "ds_read_b64_tr_b16 v[162:163], v48 offset:34816\n\t"
    "v_mfma_f32_16x16x32_bf16 v[200:203], v[164:167], v[120:123], v[200:203]\n\t"
    "buffer_load_dwordx4 v56, s[40:43], s53 offen offset:0 lds\n\t"
    "v_mfma_f32_16x16x32_bf16 v[204:207], v[164:167], v[124:127], v[204:207]\n\t"
    "ds_read_b64_tr_b16 v[164:165], v49 offset:32768\n\t"
    "ds_read_b64_tr_b16 v[166:167], v49 offset:34816\n\t"
    "v_mfma_f32_16x16x32_bf16 v[208:211], v[168:171], v[120:123], v[208:211]\n\t"
    "s_add_u32 m0, s51, 3968\n\t"
    "v_mfma_f32_16x16x32_bf16 v[212:215], v[168:171], v[124:127], v[212:215]\n\t"
    "ds_read_b64_tr_b16 v[168:169], v50 offset:32768\n\t"
    "ds_read_b64_tr_b16 v[170:171], v50 offset:34816\n\t"
    "v_mfma_f32_16x16x32_bf16 v[216:219], v[172:175], v[120:123], v[216:219]\n\t"
    "buffer_load_dwordx4 v56, s[40:43], s53 offen offset:128 lds\n\t"
    "v_mfma_f32_16x16x32_bf16 v[220:223], v[172:175], v[124:127], v[220:223]\n\t"
    "ds_read_b64_tr_b16 v[172:173], v51 offset:32768\n\t"
    "ds_read_b64_tr_b16 v[174:175], v51 offset:34816\n\t"
    "s_waitcnt lgkmcnt(8)\n\t"
    "v_mfma_f32_16x16x32_bf16 v[224:227], v[176:179], v[120:123], v[224:227]\n\t"
    "buffer_load_dwordx4 v[112:115], v57, s[36:39], s54 offen offset:0 sc1\n\t"
    "v_mfma_f32_16x16x32_bf16 v[228:231], v[176:179], v[124:127], v[228:231]\n\t"
    "ds_read_b64_tr_b16 v[176:177], v48 offset:36864\n\t"
    "ds_read_b64_tr_b16 v[178:179], v48 offset:38912\n\t"
    "v_mfma_f32_16x16x32_bf16 v[232:235], v[180:183], v[120:123], v[232:235]\n\t"
    "buffer_load_dwordx4 v[116:119], v57, s[36:39], s54 offen offset:1024 sc1\n\t"
    "v_mfma_f32_16x16x32_bf16 v[236:239], v[180:183], v[124:127], v[236:239]\n\t"
    "ds_read_b64_tr_b16 v[180:181], v49 offset:36864\n\t"
    "ds_read_b64_tr_b16 v[182:183], v49 offset:38912\n\t"
    "v_mfma_f32_16x16x32_bf16 v[240:243], v[184:187], v[120:123], v[240:243]\n\t"
    "s_add_u32 s53, s53, 131072\n\t"
    "v_mfma_f32_16x16x32_bf16 v[244:247], v[184:187], v[124:127], v[244:247]\n\t"
    "ds_read_b64_tr_b16 v[184:185], v50 offset:36864\n\t"
    "ds_read_b64_tr_b16 v[186:187], v50 offset:38912\n\t"
    "v_mfma_f32_16x16x32_bf16 v[248:251], v[188:191], v[120:123], v[248:251]\n\t"
    "s_add_u32 s54, s54, 262144\n\t"
    "v_mfma_f32_16x16x32_bf16 v[252:255], v[188:191], v[124:127], v[252:255]\n\t"
    "ds_read_b64_tr_b16 v[188:189], v51 offset:36864\n\t"
    "ds_read_b64_tr_b16 v[190:191], v51 offset:38912\n\t"
    "s_sub_u32 s55, s55, 1\n\t"
    "s_cmp_lg_u32 s55, 0\n\t"
    "s_cbranch_scc1 LK3_%=\n\t"
    "s_waitcnt vmcnt(12)\n\t"
    "s_barrier\n\t"
    "s_waitcnt lgkmcnt(8)\n\t"
    "v_mfma_f32_16x16x32_bf16 v[192:195], v[160:163], v[128:131], v[192:195]\n\t"
    "v_mfma_f32_16x16x32_bf16 v[196:199], v[160:163], v[132:135], v[196:199]\n\t"
    "ds_read_b64_tr_b16 v[160:161], v52 offset:0\n\t"
    "ds_read_b64_tr_b16 v[162:163], v52 offset:2048\n\t"
    "v_mfma_f32_16x16x32_bf16 v[200:203], v[164:167], v[128:131], v[200:203]\n\t"
    "v_mfma_f32_16x16x32_bf16 v[204:207], v[164:167], v[132:135], v[204:207]\n\t"
    "ds_read_b64_tr_b16 v[164:165], v53 offset:0\n\t"
    "ds_read_b64_tr_b16 v[166:167], v53 offset:2048\n\t"
    "v_mfma_f32_16x16x32_bf16 v[208:211], v[168:171], v[128:131], v[208:211]\n\t"
    "v_mfma_f32_16x16x32_bf16 v[212:215], v[168:171], v[132:135], v[212:215]\n\t"
    "ds_read_b64_tr_b16 v[168:169], v54 offset:0\n\t"
    "ds_read_b64_tr_b16 v[170:171], v54 offset:2048\n\t"
    "v_mfma_f32_16x16x32_bf16 v[216:219], v[172:175], v[128:131], v[216:219]\n\t"
    "v_mfma_f32_16x16x32_bf16 v[220:223], v[172:175], v[132:135], v[220:223]\n\t"
    "ds_read_b64_tr_b16 v[172:173], v55 offset:0\n\t"
    "ds_read_b64_tr_b16 v[174:175], v55 offset:2048\n\t"
    "s_waitcnt lgkmcnt(8)\n\t"
    "v_mfma_f32_16x16x32_bf16 v[224:227], v[176:179], v[128:131], v[224:227]\n\t"
    "v_mfma_f32_16x16x32_bf16 v[228:231], v[176:179], v[132:135], v[228:231]\n\t"
    "ds_read_b64_tr_b16 v[176:177], v52 offset:4096\n\t"
    "ds_read_b64_tr_b16 v[178:179], v52 offset:6144\n\t"
    "v_mfma_f32_16x16x32_bf16 v[232:235], v[180:183], v[128:131], v[232:235]\n\t"
    "v_mfma_f32_16x16x32_bf16 v[236:239], v[180:183], v[132:135], v[236:239]\n\t"
    "ds_read_b64_tr_b16 v[180:181], v53 offset:4096\n\t"
    "ds_read_b64_tr_b16 v[182:183], v53 offset:6144\n\t"
    "v_mfma_f32_16x16x32_bf16 v[240:243], v[184:187], v[128:131], v[240:243]\n\t"
    "v_mfma_f32_16x16x32_bf16 v[244:247], v[184:187], v[132:135], v[244:247]\n\t"
    "ds_read_b64_tr_b16 v[184:185], v54 offset:4096\n\t"
    "ds_read_b64_tr_b16 v[186:187], v54 offset:6144\n\t"
    "v_mfma_f32_16x16x32_bf16 v[248:251], v[188:191], v[128:131], v[248:251]\n\t"
    "v_mfma_f32_16x16x32_bf16 v[252:255], v[188:191], v[132:135], v[252:255]\n\t"
    "ds_read_b64_tr_b16 v[188:189], v55 offset:4096\n\t"
    "ds_read_b64_tr_b16 v[190:191], v55 offset:6144\n\t"
    "s_waitcnt vmcnt(8)\n\t"
    "s_barrier\n\t"
    "s_waitcnt lgkmcnt(8)\n\t"
    "v_mfma_f32_16x16x32_bf16 v[192:195], v[160:163], v[136:139], v[192:195]\n\t"
    "v_mfma_f32_16x16x32_bf16 v[196:199], v[160:163], v[140:143], v[196:199]\n\t"
    "ds_read_b64_tr_b16 v[160:161], v52 offset:16384\n\t"
    "ds_read_b64_tr_b16 v[162:163], v52 offset:18432\n\t"
    "v_mfma_f32_16x16x32_bf16 v[200:203], v[164:167], v[136:139], v[200:203]\n\t"
    "v_mfma_f32_16x16x32_bf16 v[204:207], v[164:167], v[140:143], v[204:207]\n\t"
    "ds_read_b64_tr_b16 v[164:165], v53 offset:16384\n\t"
    "ds_read_b64_tr_b16 v[166:167], v53 offset:18432\n\t"
    "v_mfma_f32_16x16x32_bf16 v[208:211], v[168:171], v[136:139], v[208:211]\n\t"
    "v_mfma_f32_16x16x32_bf16 v[212:215], v[168:171], v[140:143], v[212:215]\n\t"
    "ds_read_b64_tr_b16 v[168:169], v54 offset:16384\n\t"
    "ds_read_b64_tr_b16 v[170:171], v54 offset:18432\n\t"
    "v_mfma_f32_16x16x32_bf16 v[216:219], v[172:175], v[136:139], v[216:219]\n\t"
    "v_mfma_f32_16x16x32_bf16 v[220:223], v[172:175], v[140:143], v[220:223]\n\t"
    "ds_read_b64_tr_b16 v[172:173], v55 offset:16384\n\t"
    "ds_read_b64_tr_b16 v[174:175], v55 offset:18432\n\t"
    "s_waitcnt lgkmcnt(8)\n\t"
    "v_mfma_f32_16x16x32_bf16 v[224:227], v[176:179], v[136:139], v[224:227]\n\t"
    "v_mfma_f32_16x16x32_bf16 v[228:231], v[176:179], v[140:143], v[228:231]\n\t"
    "ds_read_b64_tr_b16 v[176:177], v52 offset:20480\n\t"
    "ds_read_b64_tr_b16 v[178:179], v52 offset:22528\n\t"
    "v_mfma_f32_16x16x32_bf16 v[232:235], v[180:183], v[136:139], v[232:235]\n\t"
    "v_mfma_f32_16x16x32_bf16 v[236:239], v[180:183], v[140:143], v[236:239]\n\t"
    "ds_read_b64_tr_b16 v[180:181], v53 offset:20480\n\t"
    "ds_read_b64_tr_b16 v[182:183], v53 offset:22528\n\t"
    "v_mfma_f32_16x16x32_bf16 v[240:243], v[184:187], v[136:139], v[240:243]\n\t"
    "v_mfma_f32_16x16x32_bf16 v[244:247], v[184:187], v[140:143], v[244:247]\n\t"
    "ds_read_b64_tr_b16 v[184:185], v54 offset:20480\n\t"
    "ds_read_b64_tr_b16 v[186:187], v54 offset:22528\n\t"
    "v_mfma_f32_16x16x32_bf16 v[248:251], v[188:191], v[136:139], v[248:251]\n\t"
    "v_mfma_f32_16x16x32_bf16 v[252:255], v[188:191], v[140:143], v[252:255]\n\t"
    "ds_read_b64_tr_b16 v[188:189], v55 offset:20480\n\t"
    "ds_read_b64_tr_b16 v[190:191], v55 offset:22528\n\t"
    "s_waitcnt vmcnt(4)\n\t"
    "s_barrier\n\t"
    "s_waitcnt lgkmcnt(8)\n\t"
    "v_mfma_f32_16x16x32_bf16 v[192:195], v[160:163], v[144:147], v[192:195]\n\t"
    "v_mfma_f32_16x16x32_bf16 v[196:199], v[160:163], v[148:151], v[196:199]\n\t"
    "ds_read_b64_tr_b16 v[160:161], v52 offset:32768\n\t"
    "ds_read_b64_tr_b16 v[162:163], v52 offset:34816\n\t"
    "v_mfma_f32_16x16x32_bf16 v[200:203], v[164:167], v[144:147], v[200:203]\n\t"
    "v_mfma_f32_16x16x32_bf16 v[204:207], v[164:167], v[148:151], v[204:207]\n\t"
    "ds_read_b64_tr_b16 v[164:165], v53 offset:32768\n\t"
    "ds_read_b64_tr_b16 v[166:167], v53 offset:34816\n\t"
    "v_mfma_f32_16x16x32_bf16 v[208:211], v[168:171], v[144:147], v[208:211]\n\t"
    "v_mfma_f32_16x16x32_bf16 v[212:215], v[168:171], v[148:151], v[212:215]\n\t"
    "ds_read_b64_tr_b16 v[168:169], v54 offset:32768\n\t"
    "ds_read_b64_tr_b16 v[170:171], v54 offset:34816\n\t"
    "v_mfma_f32_16x16x32_bf16 v[216:219], v[172:175], v[144:147], v[216:219]\n\t"
    "v_mfma_f32_16x16x32_bf16 v[220:223], v[172:175], v[148:151], v[220:223]\n\t"
    "ds_read_b64_tr_b16 v[172:173], v55 offset:32768\n\t"
    "ds_read_b64_tr_b16 v[174:175], v55 offset:34816\n\t"
    "s_waitcnt lgkmcnt(8)\n\t"
    "v_mfma_f32_16x16x32_bf16 v[224:227], v[176:179], v[144:147], v[224:227]\n\t"
    "v_mfma_f32_16x16x32_bf16 v[228:231], v[176:179], v[148:151], v[228:231]\n\t"
    "ds_read_b64_tr_b16 v[176:177], v52 offset:36864\n\t"
    "ds_read_b64_tr_b16 v[178:179], v52 offset:38912\n\t"
    "v_mfma_f32_16x16x32_bf16 v[232:235], v[180:183], v[144:147], v[232:235]\n\t"
    "v_mfma_f32_16x16x32_bf16 v[236:239], v[180:183], v[148:151], v[236:239]\n\t"
    "ds_read_b64_tr_b16 v[180:181], v53 offset:36864\n\t"
    "ds_read_b64_tr_b16 v[182:183], v53 offset:38912\n\t"
    "v_mfma_f32_16x16x32_bf16 v[240:243], v[184:187], v[144:147], v[240:243]\n\t"
    "v_mfma_f32_16x16x32_bf16 v[244:247], v[184:187], v[148:151], v[244:247]\n\t"
    "ds_read_b64_tr_b16 v[184:185], v54 offset:36864\n\t"
    "ds_read_b64_tr_b16 v[186:187], v54 offset:38912\n\t"
    "v_mfma_f32_16x16x32_bf16 v[248:251], v[188:191], v[144:147], v[248:251]\n\t"
    "v_mfma_f32_16x16x32_bf16 v[252:255], v[188:191], v[148:151], v[252:255]\n\t"
    "ds_read_b64_tr_b16 v[188:189], v55 offset:36864\n\t"
    "ds_read_b64_tr_b16 v[190:191], v55 offset:38912\n\t"
    "global_load_dword v58, v[60:61], off sc1\n\t"
    "s_waitcnt lgkmcnt(8)\n\t"
    "v_mfma_f32_16x16x32_bf16 v[192:195], v[160:163], v[152:155], v[192:195]\n\t"
    "v_mfma_f32_16x16x32_bf16 v[196:199], v[160:163], v[156:159], v[196:199]\n\t"
    "v_mfma_f32_16x16x32_bf16 v[200:203], v[164:167], v[152:155], v[200:203]\n\t"
    "v_mfma_f32_16x16x32_bf16 v[204:207], v[164:167], v[156:159], v[204:207]\n\t"
    "v_mfma_f32_16x16x32_bf16 v[208:211], v[168:171], v[152:155], v[208:211]\n\t"
    "v_mfma_f32_16x16x32_bf16 v[212:215], v[168:171], v[156:159], v[212:215]\n\t"
    "v_mfma_f32_16x16x32_bf16 v[216:219], v[172:175], v[152:155], v[216:219]\n\t"
    "v_mfma_f32_16x16x32_bf16 v[220:223], v[172:175], v[156:159], v[220:223]\n\t"
    "s_waitcnt lgkmcnt(0)\n\t"
    "v_mfma_f32_16x16x32_bf16 v[224:227], v[176:179], v[152:155], v[224:227]\n\t"
    "v_mfma_f32_16x16x32_bf16 v[228:231], v[176:179], v[156:159], v[228:231]\n\t"
    "v_mfma_f32_16x16x32_bf16 v[232:235], v[180:183], v[152:155], v[232:235]\n\t"
    "v_mfma_f32_16x16x32_bf16 v[236:239], v[180:183], v[156:159], v[236:239]\n\t"
    "v_mfma_f32_16x16x32_bf16 v[240:243], v[184:187], v[152:155], v[240:243]\n\t"
    "v_mfma_f32_16x16x32_bf16 v[244:247], v[184:187], v[156:159], v[244:247]\n\t"
    "v_mfma_f32_16x16x32_bf16 v[248:251], v[188:191], v[152:155], v[248:251]\n\t"
    "v_mfma_f32_16x16x32_bf16 v[252:255], v[188:191], v[156:159], v[252:255]\n\t"
    "s_branch LK3END_%=\n\t"
    "LK3PLA_%=:\n\t"
    "global_load_dword v59, v[62:63], off sc1\n\t"
    "s_branch LK3PBA_%=\n\t"
    "LK3SWA_%=:\n\t"
    "s_mov_b32 s62, 0x40000\n\t"
    "LK3CKA_%=:\n\t"
    "v_readfirstlane_b32 s63, v59\n\t"
    "s_nop 3\n\t"
    "s_cmp_ge_u32 s63, 16\n\t"
    "s_cbranch_scc1 LK3OKA_%=\n\t"
    "s_sleep 2\n\t"
    "global_load_dword v59, v[62:63], off sc1\n\t"
    "s_waitcnt vmcnt(0)\n\t"
    "s_sub_u32 s62, s62, 1\n\t"
    "s_cmp_lg_u32 s62, 0\n\t"
    "s_cbranch_scc1 LK3CKA_%=\n\t"
    "LK3OKA_%=:\n\t"
    "s_mov_b32 s40, s44\n\t"
    "s_mov_b32 s41, s45\n\t"
    "s_mov_b32 s42, s46\n\t"
    "s_mov_b32 s53, s75\n\t"
    "s_mov_b32 s54, s74\n\t"
    "s_branch LK3SBA_%=\n\t"
    "LK3PLB_%=:\n\t"
    "global_load_dword v59, v[62:63], off sc1\n\t"
    "s_branch LK3PBB_%=\n\t"
    "LK3SWB_%=:\n\t"
    "s_mov_b32 s62, 0x40000\n\t"
    "LK3CKB_%=:\n\t"
    "v_readfirstlane_b32 s63, v59\n\t"
    "s_nop 3\n\t"
    "s_cmp_ge_u32 s63, 16\n\t"
    "s_cbranch_scc1 LK3OKB_%=\n\t"
    "s_sleep 2\n\t"
    "global_load_dword v59, v[62:63], off sc1\n\t"
    "s_waitcnt vmcnt(0)\n\t"
    "s_sub_u32 s62, s62, 1\n\t"
    "s_cmp_lg_u32 s62, 0\n\t"
    "s_cbranch_scc1 LK3CKB_%=\n\t"
    "LK3OKB_%=:\n\t"
    "s_mov_b32 s40, s44\n\t"
    "s_mov_b32 s41, s45\n\t"
    "s_mov_b32 s42, s46\n\t"
    "s_mov_b32 s53, s75\n\t"
    "s_mov_b32 s54, s74\n\t"
    "s_branch LK3SBB_%=\n\t"
    "LK3END_%=:\n\t"
    "s_waitcnt vmcnt(0) lgkmcnt(0)\n\t"
    "s_barrier\n\t"
    "s_nop 7\n\t"
    "s_nop 7\n\t"
    : "={v[192:207]}"(c0), "={v[208:223]}"(c1), "={v[224:239]}"(c2), "={v[240:255]}"(c3), "={v58}"(seen), "+{v59}"(hseen), "+{s[36:51]}"(sg)
    : "{s[72:79]}"(st8), "{v[48:55]}"(va), "{v[56:57]}"(vg), "{v[60:61]}"(flag), "{v[62:63]}"(ocp), "{v[64:65]}"(osig), "{v[66:67]}"(hcp), "{s64}"(wid)
    : "v112","v113","v114","v115","v116","v117","v118","v119","v120","v121","v122","v123","v124","v125","v126","v127","v128","v129","v130","v131","v132","v133","v134","v135","v136","v137","v138","v139","v140","v141","v142","v143","v144","v145","v146","v147","v148","v149","v150","v151","v152","v153","v154","v155","v156","v157","v158","v159","v160","v161","v162","v163","v164","v165","v166","v167","v168","v169","v170","v171","v172","v173","v174","v175","v176","v177","v178","v179","v180","v181","v182","v183","v184","v185","v186","v187","v188","v189","v190","v191","s52","s53","s54","s55","s56","s57","s58","s59","s62","s63", "memory", "scc");
  GAS unsigned* tmo = (GAS unsigned*)(ws + WS_FLAGS) + FL_TMO;
  const __amdgpu_buffer_rsrc_t rE = __builtin_amdgcn_make_buffer_rsrc(ws + WS_EX, 0, 4 << 20, 0x00020000);
  const int slab = (pair * 8 + wid) * 4;
  {
    LAS f32x4* dst = (LAS f32x4*)(size_t)(lb + kg * (128 * 528) + (32 * w + (lane & 15)) * 528 + g * 16);
#define K3_PUT(J, C, O) dst[(J) * 4] = (f32x4){C[O], C[O + 1], C[O + 2], C[O + 3]}; dst[(J) * 4 + 16 * 33] = (f32x4){C[O + 4], C[O + 5], C[O + 6], C[O + 7]};
    K3_PUT(0, c0, 0) K3_PUT(1, c0, 8) K3_PUT(2, c1, 0) K3_PUT(3, c1, 8) K3_PUT(4, c2, 0) K3_PUT(5, c2, 8) K3_PUT(6, c3, 0) K3_PUT(7, c3, 8)
  }
  u32x4 o[4];
  if (kh) {
    for (unsigned spins = 0; !__builtin_amdgcn_readfirstlane(seen);) {
      if (spin_give_up(spins, tmo)) break;
      seen = __hip_atomic_load(flag, __ATOMIC_RELAXED, __HIP_MEMORY_SCOPE_AGENT);
    }
#pragma unroll
    for (int i = 0; i < 4; ++i) o[i] = __builtin_amdgcn_raw_buffer_load_b128(rE, (slab + i) * 1024 + lane * 16, 0, 16);
  }
  __syncthreads();
  f32x4 m[4][2];
  {
    const LAS f32x4* s0 = (const LAS f32x4*)(size_t)(lb + (16 * wid + (lane >> 4)) * 528 + (lane & 15) * 32);
    const LAS f32x4* s1 = (const LAS f32x4*)(size_t)(lb + 128 * 528 + (16 * wid + (lane >> 4)) * 528 + (lane & 15) * 32);
#pragma unroll
    for (int i = 0; i < 4; ++i) { m[i][0] = s0[i * 4 * 33] + s1[i * 4 * 33]; m[i][1] = s0[i * 4 * 33 + 1] + s1[i * 4 * 33 + 1]; }
  }
  if (!kh) {
#pragma unroll
    for (int i = 0; i < 4; ++i)
      __builtin_amdgcn_raw_buffer_store_b128((u32x4){cvtpk(m[i][0][0], m[i][0][1]), cvtpk(m[i][0][2], m[i][0][3]), cvtpk(m[i][1][0], m[i][1][1]), cvtpk(m[i][1][2], m[i][1][3])},
                                             rE, (slab + i) * 1024 + lane * 16, 0, 16);
    asm volatile("s_waitcnt vmcnt(0)" ::: "memory");
    __syncthreads();
    if (tid == 0) __hip_atomic_store(flag, 1u, __ATOMIC_RELAXED, __HIP_MEMORY_SCOPE_AGENT);
  } else {
    const __amdgpu_buffer_rsrc_t rO = __builtin_amdgcn_make_buffer_rsrc(Aout, 0, S_ * D_ * 2, 0x00020000);
#pragma unroll
    for (int i = 0; i < 4; ++i) {
      u32x4 r;
#pragma unroll
      for (int q = 0; q < 4; ++q)
        r[q] = cvtpk(bflo(xr[i][q]) + (m[i][q >> 1][2 * (q & 1)] + bflo(o[i][q])), bfhi(xr[i][q]) + (m[i][q >> 1][2 * (q & 1) + 1] + bfhi(o[i][q])));
      __builtin_amdgcn_raw_buffer_store_b128(r, rO, (unsigned)(orow * 2) + i * 4 * D_ * 2, 0, OUT_AUX);
    }
  }
}

#define K2_NOX 0
#define K2_NOW 0
#define WARM 2
#define K2_SET_SLOT(S, PTR, LD, PIECE, VOFF) do { const unsigned long long wp_ = (unsigned long long)(PTR); \
    sg[4 + 4 * (S)] = (unsigned)wp_; sg[5 + 4 * (S)] = (unsigned)(wp_ >> 32) & 0xffffu; sg[6 + 4 * (S)] = K2_NOW ? 0u : 1024u * (LD); sg[7 + 4 * (S)] = 0x00020000u; \
    sp[S] = 32 * (LD); sp[3 + (S)] = lb + 16384 + (PIECE) * 1024; vg[7 + (S)] = (VOFF); } while (0)
#define K2_TOUCH_SLOT(N1, N2, CB1, CB2) do { const int i_ = panel * 4 + tg, cls_ = (i_ >= (N1)) + (i_ >= (N1) + (N2)), ii_ = i_ - (cls_ >= 1 ? (N1) : 0) - (cls_ == 2 ? (N2) : 0); \
    const int ld_ = cls_ == 0 ? 6144 : 7168, ra_ = (ii_ & 3) * 8 + (lane >> 3); \
    const int oa_ = ra_ * ld_ + (cls_ == 0 ? (CB1) + (ii_ >> 2) * 2048 : (CB2) + (ii_ >> 2) * 128) + (lane & 7) * 16; \
    const int u_ = ii_ * 64 + lane, rb_ = (u_ * 5462) >> 16, ob_ = rb_ * 7168 + (CB2) + (u_ - rb_ * 12) * 16; \
    K2_SET_SLOT(2, cls_ == 0 ? wqkv : (cls_ == 1 ? wg : wu), ld_, 0, ((N2) == 6 && cls_ > 0) ? ob_ : oa_); \
    if (i_ >= 20) sg[14] = 0; \
    sp[5] = lb + K2_KVF + tg * 1024; } while (0)
#define K2_ASM(LOOP) \
    f32x16 a0, a1, a2, a3, a4, a5, a6, a7, a8, a9; \
    asm volatile(LOOP \
    : "={v[96:111]}"(a0), "={v[112:127]}"(a1), "={v[128:143]}"(a2), "={v[144:159]}"(a3), "={v[160:175]}"(a4), \
      "={v[176:191]}"(a5), "={v[192:207]}"(a6), "={v[208:223]}"(a7), "={v[224:239]}"(a8), "={v[240:255]}"(a9), \
      "+{s[36:51]}"(sg), "+{s[52:59]}"(sp), "+{v[28:43]}"(vg) \
    : "{s[68:71]}"(sq), "{s72}"(scr), "{s73}"(pfon), "{v[24:25]}"(pfp) \
    : "v44","v45","v46","v47","v48","v49","v50","v51","v52","v53","v54","v55","v56","v57","v58","v59","v60","v61","v62","v63","v64","v65","v66","v67","v68","v69","v70","v71","v72","v73","v74","v75","v76","v77","v78","v79","v80","v81","v82","v83","v84","v85","v86","v87","v88","v89","v90","v91","v92","v93","v94","v95","s60","s61","s62","s63","s64", "memory", "scc"); \
    int tid = threadIdx.x; asm volatile("" : "+v"(tid)); \
    const int lane = tid & 63, g = lane >> 4, q4 = (lane >> 2) & 3, p4 = lane & 3; \
    const int head = 2 * pr + cg; \
    const int tt0 = panel * 16 + tg * 4; \
    const __amdgpu_buffer_rsrc_t rA2 = __builtin_amdgcn_make_buffer_rsrc(ws + WS_A2, 0, (int)A2_BYTES, 0x00020000); \
    const unsigned trl = (4 * (g & 1) + q4) * 32 + 8 * p4; \
    const unsigned wb = lb + K2_WPHI + cg * 2048 + (g >> 1) * 512 + trl; \
    const bf16x8 w0 = tr_frag(wb, wb + 1024), w1 = tr_frag(wb + 256, wb + 256 + 1024); \
    GAS unsigned* flags = (GAS unsigned*)(ws + WS_FLAGS); GAS unsigned* tmo = flags + FL_TMO; \
    const __amdgpu_buffer_rsrc_t rK = __builtin_amdgcn_make_buffer_rsrc(ws + WS_KVP, 0, KVP_BYTES, 0x00020000);
#define K2_LOOP_X \
    "s_cmp_eq_u32 s59, 1\n\t" \
    "s_cbranch_scc1 LK2V1_%=\n\t" \
    "s_nop 4\n\t" \
    "s_mov_b32 s60, 0\n\t" \
    "s_mov_b32 s61, 0\n\t" \
    "s_mov_b32 s62, 0\n\t" \
    "s_mov_b32 s63, 0\n\t" \
    "s_add_u32 m0, s58, 0\n\t" \
    "s_nop 0\n\t" \
    "buffer_load_dwordx4 v38, s[36:39], s60 offen lds\n\t" \
    "v_mov_b32 v96, 0\n\t" \
    "v_mov_b32 v97, 0\n\t" \
    "v_mov_b32 v98, 0\n\t" \
    "v_mov_b32 v99, 0\n\t" \
    "v_mov_b32 v100, 0\n\t" \
    "v_mov_b32 v101, 0\n\t" \
    "v_mov_b32 v102, 0\n\t" \
    "v_mov_b32 v103, 0\n\t" \
    "v_mov_b32 v104, 0\n\t" \
    "v_mov_b32 v105, 0\n\t" \
    "v_mov_b32 v106, 0\n\t" \
    "v_mov_b32 v107, 0\n\t" \
    "v_mov_b32 v108, 0\n\t" \
    "v_mov_b32 v109, 0\n\t" \
    "v_mov_b32 v110, 0\n\t" \
    "v_mov_b32 v111, 0\n\t" \
    "buffer_load_dwordx4 v38, s[36:39], s60 offen offset:1024 lds\n\t" \
    "v_mov_b32 v112, 0\n\t" \
    "v_mov_b32 v113, 0\n\t" \
    "v_mov_b32 v114, 0\n\t" \
    "v_mov_b32 v115, 0\n\t" \
    "v_mov_b32 v116, 0\n\t" \
    "v_mov_b32 v117, 0\n\t" \
    "v_mov_b32 v118, 0\n\t" \
    "v_mov_b32 v119, 0\n\t" \
    "v_mov_b32 v120, 0\n\t" \
    "v_mov_b32 v121, 0\n\t" \
    "v_mov_b32 v122, 0\n\t" \
    "v_mov_b32 v123, 0\n\t" \
    "v_mov_b32 v124, 0\n\t" \
    "v_mov_b32 v125, 0\n\t" \
    "v_mov_b32 v126, 0\n\t" \
    "v_mov_b32 v127, 0\n\t" \
    "s_add_u32 m0, s55, 0\n\t" \
    "s_nop 0\n\t" \
    "buffer_load_dwordx4 v35, s[40:43], s61 offen lds\n\t" \
    "v_mov_b32 v128, 0\n\t" \
    "v_mov_b32 v129, 0\n\t" \
    "v_mov_b32 v130, 0\n\t" \
    "v_mov_b32 v131, 0\n\t" \
    "v_mov_b32 v132, 0\n\t" \
    "v_mov_b32 v133, 0\n\t" \
    "v_mov_b32 v134, 0\n\t" \
    "v_mov_b32 v135, 0\n\t" \
    "v_mov_b32 v136, 0\n\t" \
    "v_mov_b32 v137, 0\n\t" \
    "v_mov_b32 v138, 0\n\t" \
    "v_mov_b32 v139, 0\n\t" \
    "v_mov_b32 v140, 0\n\t" \
    "v_mov_b32 v141, 0\n\t" \
    "v_mov_b32 v142, 0\n\t" \
    "v_mov_b32 v143, 0\n\t" \
    "s_add_u32 m0, s56, 0\n\t" \
    "s_nop 0\n\t" \
    "buffer_load_dwordx4 v36, s[44:47], s62 offen lds\n\t" \
    "v_mov_b32 v144, 0\n\t" \
    "v_mov_b32 v145, 0\n\t" \
    "v_mov_b32 v146, 0\n\t" \
    "v_mov_b32 v147, 0\n\t" \
    "v_mov_b32 v148, 0\n\t" \
    "v_mov_b32 v149, 0\n\t" \
    "v_mov_b32 v150, 0\n\t" \
    "v_mov_b32 v151, 0\n\t" \
    "v_mov_b32 v152, 0\n\t" \
    "v_mov_b32 v153, 0\n\t" \
    "v_mov_b32 v154, 0\n\t" \
    "v_mov_b32 v155, 0\n\t" \
    "v_mov_b32 v156, 0\n\t" \
    "v_mov_b32 v157, 0\n\t" \
    "v_mov_b32 v158, 0\n\t" \
    "v_mov_b32 v159, 0\n\t" \
    "s_add_u32 m0, s57, 0\n\t" \
    "s_nop 0\n\t" \
    "buffer_load_dwordx4 v37, s[48:51], s63 offen lds\n\t" \
    "v_mov_b32 v160, 0\n\t" \
    "v_mov_b32 v161, 0\n\t" \
    "v_mov_b32 v162, 0\n\t" \
    "v_mov_b32 v163, 0\n\t" \
    "v_mov_b32 v164, 0\n\t" \
    "v_mov_b32 v165, 0\n\t" \
    "v_mov_b32 v166, 0\n\t" \
    "v_mov_b32 v167, 0\n\t" \
    "v_mov_b32 v168, 0\n\t" \
    "v_mov_b32 v169, 0\n\t" \
    "v_mov_b32 v170, 0\n\t" \
    "v_mov_b32 v171, 0\n\t" \
    "v_mov_b32 v172, 0\n\t" \
    "v_mov_b32 v173, 0\n\t" \
    "v_mov_b32 v174, 0\n\t" \
    "v_mov_b32 v175, 0\n\t" \
    "s_add_u32 s60, s60, 131072\n\t" \
    "s_add_u32 s61, s61, s52\n\t" \
    "s_add_u32 s62, s62, s53\n\t" \
    "s_add_u32 s63, s63, s54\n\t" \
    "s_add_u32 m0, s58, 36864\n\t" \
    "s_nop 0\n\t" \
    "buffer_load_dwordx4 v38, s[36:39], s60 offen lds\n\t" \
    "v_mov_b32 v176, 0\n\t" \
    "v_mov_b32 v177, 0\n\t" \
    "v_mov_b32 v178, 0\n\t" \
    "v_mov_b32 v179, 0\n\t" \
    "v_mov_b32 v180, 0\n\t" \
    "v_mov_b32 v181, 0\n\t" \
    "v_mov_b32 v182, 0\n\t" \
    "v_mov_b32 v183, 0\n\t" \
    "v_mov_b32 v184, 0\n\t" \
    "v_mov_b32 v185, 0\n\t" \
    "v_mov_b32 v186, 0\n\t" \
    "v_mov_b32 v187, 0\n\t" \
    "v_mov_b32 v188, 0\n\t" \
    "v_mov_b32 v189, 0\n\t" \
    "v_mov_b32 v190, 0\n\t" \
    "v_mov_b32 v191, 0\n\t" \
    "buffer_load_dwordx4 v38, s[36:39], s60 offen offset:1024 lds\n\t" \
    "v_mov_b32 v192, 0\n\t" \
    "v_mov_b32 v193, 0\n\t" \
    "v_mov_b32 v194, 0\n\t" \
    "v_mov_b32 v195, 0\n\t" \
    "v_mov_b32 v196, 0\n\t" \
    "v_mov_b32 v197, 0\n\t" \
    "v_mov_b32 v198, 0\n\t" \
    "v_mov_b32 v199, 0\n\t" \
    "v_mov_b32 v200, 0\n\t" \
    "v_mov_b32 v201, 0\n\t" \
    "v_mov_b32 v202, 0\n\t" \
    "v_mov_b32 v203, 0\n\t" \
    "v_mov_b32 v204, 0\n\t" \
    "v_mov_b32 v205, 0\n\t" \
    "v_mov_b32 v206, 0\n\t" \
    "v_mov_b32 v207, 0\n\t" \
    "s_add_u32 m0, s55, 36864\n\t" \
    "s_nop 0\n\t" \
    "buffer_load_dwordx4 v35, s[40:43], s61 offen lds\n\t" \
    "v_mov_b32 v208, 0\n\t" \
    "v_mov_b32 v209, 0\n\t" \
    "v_mov_b32 v210, 0\n\t" \
    "v_mov_b32 v211, 0\n\t" \
    "v_mov_b32 v212, 0\n\t" \
    "v_mov_b32 v213, 0\n\t" \
    "v_mov_b32 v214, 0\n\t" \
    "v_mov_b32 v215, 0\n\t" \
    "v_mov_b32 v216, 0\n\t" \
    "v_mov_b32 v217, 0\n\t" \
    "v_mov_b32 v218, 0\n\t" \
    "v_mov_b32 v219, 0\n\t" \
    "v_mov_b32 v220, 0\n\t" \
    "v_mov_b32 v221, 0\n\t" \
    "v_mov_b32 v222, 0\n\t" \
    "v_mov_b32 v223, 0\n\t" \
    "s_add_u32 m0, s56, 36864\n\t" \
    "s_nop 0\n\t" \
    "buffer_load_dwordx4 v36, s[44:47], s62 offen lds\n\t" \
    "v_mov_b32 v224, 0\n\t" \
    "v_mov_b32 v225, 0\n\t" \
    "v_mov_b32 v226, 0\n\t" \
    "v_mov_b32 v227, 0\n\t" \
    "v_mov_b32 v228, 0\n\t" \
    "v_mov_b32 v229, 0\n\t" \
    "v_mov_b32 v230, 0\n\t" \
    "v_mov_b32 v231, 0\n\t" \
    "v_mov_b32 v232, 0\n\t" \
    "v_mov_b32 v233, 0\n\t" \
    "v_mov_b32 v234, 0\n\t" \
    "v_mov_b32 v235, 0\n\t" \
    "v_mov_b32 v236, 0\n\t" \
    "v_mov_b32 v237, 0\n\t" \
    "v_mov_b32 v238, 0\n\t" \
    "v_mov_b32 v239, 0\n\t" \
    "s_add_u32 m0, s57, 36864\n\t" \
    "s_nop 0\n\t" \
    "buffer_load_dwordx4 v37, s[48:51], s63 offen lds\n\t" \
    "v_mov_b32 v240, 0\n\t" \
    "v_mov_b32 v241, 0\n\t" \
    "v_mov_b32 v242, 0\n\t" \
    "v_mov_b32 v243, 0\n\t" \
    "v_mov_b32 v244, 0\n\t" \
    "v_mov_b32 v245, 0\n\t" \
    "v_mov_b32 v246, 0\n\t" \
    "v_mov_b32 v247, 0\n\t" \
    "v_mov_b32 v248, 0\n\t" \
    "v_mov_b32 v249, 0\n\t" \
    "v_mov_b32 v250, 0\n\t" \
    "v_mov_b32 v251, 0\n\t" \
    "v_mov_b32 v252, 0\n\t" \
    "v_mov_b32 v253, 0\n\t" \
    "v_mov_b32 v254, 0\n\t" \
    "v_mov_b32 v255, 0\n\t" \
    "s_add_u32 s60, s60, 131072\n\t" \
    "s_add_u32 s61, s61, s52\n\t" \
    "s_add_u32 s62, s62, s53\n\t" \
    "s_add_u32 s63, s63, s54\n\t" \
    "s_mov_b32 s64, 8\n\t" \
    "s_waitcnt vmcnt(5)\n\t" \
    "s_barrier\n\t" \
    "ds_read_b128 v[64:67], v34 offset:0\n\t" \
    "ds_read_b128 v[68:71], v34 offset:1024\n\t" \
    "ds_read_b128 v[72:75], v34 offset:2048\n\t" \
    "ds_read_b128 v[76:79], v34 offset:3072\n\t" \
    "ds_read_b64_tr_b16 v[44:45], v28 offset:0\n\t" \
    "ds_read_b64_tr_b16 v[46:47], v28 offset:2048\n\t" \
    "ds_read_b64_tr_b16 v[48:49], v29 offset:0\n\t" \
    "ds_read_b64_tr_b16 v[50:51], v29 offset:2048\n\t" \
    "ds_read_b64_tr_b16 v[52:53], v28 offset:4096\n\t" \
    "ds_read_b64_tr_b16 v[54:55], v28 offset:6144\n\t" \
    "ds_read_b64_tr_b16 v[56:57], v29 offset:4096\n\t" \
    "ds_read_b64_tr_b16 v[58:59], v29 offset:6144\n\t" \
    "s_add_u32 m0, s58, 73728\n\t" \
    "s_nop 0\n\t" \
    "buffer_load_dwordx4 v38, s[36:39], s60 offen lds\n\t" \
    "buffer_load_dwordx4 v38, s[36:39], s60 offen offset:1024 lds\n\t" \
    "s_add_u32 m0, s55, 73728\n\t" \
    "s_nop 0\n\t" \
    "buffer_load_dwordx4 v35, s[40:43], s61 offen lds\n\t" \
    "s_add_u32 m0, s56, 73728\n\t" \
    "s_nop 0\n\t" \
    "buffer_load_dwordx4 v36, s[44:47], s62 offen lds\n\t" \
    "s_add_u32 m0, s57, 73728\n\t" \
    "s_nop 0\n\t" \
    "buffer_load_dwordx4 v37, s[48:51], s63 offen lds\n\t" \
    "s_add_u32 s60, s60, 131072\n\t" \
    "s_add_u32 s61, s61, s52\n\t" \
    "s_add_u32 s62, s62, s53\n\t" \
    "s_add_u32 s63, s63, s54\n\t" \
    "s_cmp_eq_u32 s73, 0\n\t" \
    "s_cbranch_scc1 LK2NP_v0_%=\n\t" \
    "s_add_u32 m0, s72, 155648\n\t" \
    "s_nop 0\n\t" \
    "global_load_lds_dword v[24:25], off\n\t" \
    "LK2NP_v0_%=:\n\t" \
    "LK2_v0_%=:\n\t" \
    "s_waitcnt lgkmcnt(6)\n\t" \
    "v_mfma_f32_16x16x32_bf16 v[96:99], v[44:47], v[64:67], v[96:99]\n\t" \
    "s_add_u32 m0, s58, 110592\n\t" \
    "v_mfma_f32_16x16x32_bf16 v[100:103], v[44:47], v[68:71], v[100:103]\n\t" \
    "ds_read_b64_tr_b16 v[60:61], v30 offset:0\n\t" \
    "v_mfma_f32_16x16x32_bf16 v[104:107], v[44:47], v[72:75], v[104:107]\n\t" \
    "buffer_load_dwordx4 v38, s[36:39], s60 offen lds\n\t" \
    "v_mfma_f32_16x16x32_bf16 v[108:111], v[44:47], v[76:79], v[108:111]\n\t" \
    "ds_read_b64_tr_b16 v[62:63], v30 offset:3072\n\t" \
    "s_waitcnt lgkmcnt(6)\n\t" \
    "v_mfma_f32_16x16x32_bf16 v[112:115], v[48:51], v[64:67], v[112:115]\n\t" \
    "ds_read_b64_tr_b16 v[44:45], v30 offset:6144\n\t" \
    "v_mfma_f32_16x16x32_bf16 v[116:119], v[48:51], v[68:71], v[116:119]\n\t" \
    "buffer_load_dwordx4 v38, s[36:39], s60 offen offset:1024 lds\n\t" \
    "v_mfma_f32_16x16x32_bf16 v[120:123], v[48:51], v[72:75], v[120:123]\n\t" \
    "ds_read_b64_tr_b16 v[46:47], v30 offset:9216\n\t" \
    "v_mfma_f32_16x16x32_bf16 v[124:127], v[48:51], v[76:79], v[124:127]\n\t" \
    "s_waitcnt lgkmcnt(6)\n\t" \
    "v_mfma_f32_16x16x32_bf16 v[128:131], v[52:55], v[64:67], v[128:131]\n\t" \
    "s_add_u32 m0, s55, 110592\n\t" \
    "v_mfma_f32_16x16x32_bf16 v[132:135], v[52:55], v[68:71], v[132:135]\n\t" \
    "ds_read_b64_tr_b16 v[48:49], v31 offset:32\n\t" \
    "v_mfma_f32_16x16x32_bf16 v[136:139], v[52:55], v[72:75], v[136:139]\n\t" \
    "buffer_load_dwordx4 v35, s[40:43], s61 offen lds\n\t" \
    "v_mfma_f32_16x16x32_bf16 v[140:143], v[52:55], v[76:79], v[140:143]\n\t" \
    "ds_read_b64_tr_b16 v[50:51], v31 offset:3104\n\t" \
    "s_waitcnt lgkmcnt(6)\n\t" \
    "v_mfma_f32_16x16x32_bf16 v[144:147], v[56:59], v[64:67], v[144:147]\n\t" \
    "s_add_u32 m0, s56, 110592\n\t" \
    "v_mfma_f32_16x16x32_bf16 v[148:151], v[56:59], v[68:71], v[148:151]\n\t" \
    "ds_read_b64_tr_b16 v[52:53], v31 offset:6176\n\t" \
    "v_mfma_f32_16x16x32_bf16 v[152:155], v[56:59], v[72:75], v[152:155]\n\t" \
    "buffer_load_dwordx4 v36, s[44:47], s62 offen lds\n\t" \
    "v_mfma_f32_16x16x32_bf16 v[156:159], v[56:59], v[76:79], v[156:159]\n\t" \
    "ds_read_b64_tr_b16 v[54:55], v31 offset:9248\n\t" \
    "s_waitcnt lgkmcnt(6)\n\t" \
    "v_mfma_f32_16x16x32_bf16 v[160:163], v[60:63], v[64:67], v[160:163]\n\t" \
    "s_add_u32 m0, s57, 110592\n\t" \
    "v_mfma_f32_16x16x32_bf16 v[164:167], v[60:63], v[68:71], v[164:167]\n\t" \
    "ds_read_b64_tr_b16 v[56:57], v30 offset:64\n\t" \
    "v_mfma_f32_16x16x32_bf16 v[168:171], v[60:63], v[72:75], v[168:171]\n\t" \
    "buffer_load_dwordx4 v37, s[48:51], s63 offen lds\n\t" \
    "v_mfma_f32_16x16x32_bf16 v[172:175], v[60:63], v[76:79], v[172:175]\n\t" \
    "ds_read_b64_tr_b16 v[58:59], v30 offset:3136\n\t" \
    "s_waitcnt lgkmcnt(6)\n\t" \
    "v_mfma_f32_16x16x32_bf16 v[176:179], v[44:47], v[64:67], v[176:179]\n\t" \
    "ds_read_b64_tr_b16 v[60:61], v30 offset:6208\n\t" \
    "ds_read_b64_tr_b16 v[62:63], v30 offset:9280\n\t" \
    "v_mfma_f32_16x16x32_bf16 v[180:183], v[44:47], v[68:71], v[180:183]\n\t" \
    "s_add_u32 s60, s60, 131072\n\t" \
    "v_mfma_f32_16x16x32_bf16 v[184:187], v[44:47], v[72:75], v[184:187]\n\t" \
    "s_add_u32 s61, s61, s52\n\t" \
    "s_add_u32 s62, s62, s53\n\t" \
    "v_mfma_f32_16x16x32_bf16 v[188:191], v[44:47], v[76:79], v[188:191]\n\t" \
    "s_add_u32 s63, s63, s54\n\t" \
    "s_waitcnt lgkmcnt(6)\n\t" \
    "v_mfma_f32_16x16x32_bf16 v[192:195], v[48:51], v[64:67], v[192:195]\n\t" \
    "v_mfma_f32_16x16x32_bf16 v[196:199], v[48:51], v[68:71], v[196:199]\n\t" \
    "v_mfma_f32_16x16x32_bf16 v[200:203], v[48:51], v[72:75], v[200:203]\n\t" \
    "v_mfma_f32_16x16x32_bf16 v[204:207], v[48:51], v[76:79], v[204:207]\n\t" \
    "s_waitcnt vmcnt(10) lgkmcnt(0)\n\t" \
    "s_barrier\n\t" \
    "v_mfma_f32_16x16x32_bf16 v[208:211], v[52:55], v[64:67], v[208:211]\n\t" \
    "ds_read_b128 v[80:83], v34 offset:36864\n\t" \
    "ds_read_b64_tr_b16 v[44:45], v28 offset:36864\n\t" \
    "v_mfma_f32_16x16x32_bf16 v[212:215], v[52:55], v[68:71], v[212:215]\n\t" \
    "ds_read_b64_tr_b16 v[46:47], v28 offset:38912\n\t" \
    "v_mfma_f32_16x16x32_bf16 v[216:219], v[52:55], v[72:75], v[216:219]\n\t" \
    "ds_read_b128 v[84:87], v34 offset:37888\n\t" \
    "ds_read_b128 v[88:91], v34 offset:38912\n\t" \
    "v_mfma_f32_16x16x32_bf16 v[220:223], v[52:55], v[76:79], v[220:223]\n\t" \
    "ds_read_b128 v[92:95], v34 offset:39936\n\t" \
    "v_mfma_f32_16x16x32_bf16 v[224:227], v[56:59], v[64:67], v[224:227]\n\t" \
    "ds_read_b64_tr_b16 v[48:49], v29 offset:36864\n\t" \
    "v_mfma_f32_16x16x32_bf16 v[228:231], v[56:59], v[68:71], v[228:231]\n\t" \
    "ds_read_b64_tr_b16 v[50:51], v29 offset:38912\n\t" \
    "v_mfma_f32_16x16x32_bf16 v[232:235], v[56:59], v[72:75], v[232:235]\n\t" \
    "ds_read_b64_tr_b16 v[52:53], v28 offset:40960\n\t" \
    "v_mfma_f32_16x16x32_bf16 v[236:239], v[56:59], v[76:79], v[236:239]\n\t" \
    "ds_read_b64_tr_b16 v[54:55], v28 offset:43008\n\t" \
    "v_mfma_f32_16x16x32_bf16 v[240:243], v[60:63], v[64:67], v[240:243]\n\t" \
    "ds_read_b64_tr_b16 v[56:57], v29 offset:40960\n\t" \
    "v_mfma_f32_16x16x32_bf16 v[244:247], v[60:63], v[68:71], v[244:247]\n\t" \
    "v_mfma_f32_16x16x32_bf16 v[248:251], v[60:63], v[72:75], v[248:251]\n\t" \
    "ds_read_b64_tr_b16 v[58:59], v29 offset:43008\n\t" \
    "v_mfma_f32_16x16x32_bf16 v[252:255], v[60:63], v[76:79], v[252:255]\n\t" \
    "s_cmp_eq_u32 s64, 1\n\t" \
    "s_cbranch_scc0 LK2PF1_v0_%=\n\t" \
    "s_mov_b32 s40, s68\n\t" \
    "s_mov_b32 s41, s69\n\t" \
    "s_mov_b32 s42, 7340032\n\t" \
    "s_mov_b32 s52, 4194304\n\t" \
    "s_mov_b32 s61, 0\n\t" \
    "v_mov_b32 v35, v39\n\t" \
    "s_mov_b32 s55, s72\n\t" \
    "s_mov_b32 s44, s68\n\t" \
    "s_mov_b32 s45, s69\n\t" \
    "s_mov_b32 s46, 7340032\n\t" \
    "s_mov_b32 s53, 4194304\n\t" \
    "s_mov_b32 s62, 2097152\n\t" \
    "v_mov_b32 v36, v39\n\t" \
    "s_mov_b32 s56, s72\n\t" \
    "s_mov_b32 s48, s70\n\t" \
    "s_mov_b32 s49, s71\n\t" \
    "s_mov_b32 s50, 2097152\n\t" \
    "s_mov_b32 s54, 1048576\n\t" \
    "s_mov_b32 s63, 0\n\t" \
    "v_mov_b32 v37, v40\n\t" \
    "s_mov_b32 s57, s72\n\t" \
    "s_mov_b32 s58, s72\n\t" \
    "LK2PF1_v0_%=:\n\t" \
    "s_waitcnt lgkmcnt(6)\n\t" \
    "v_mfma_f32_16x16x32_bf16 v[96:99], v[44:47], v[80:83], v[96:99]\n\t" \
    "s_add_u32 m0, s58, 0\n\t" \
    "v_mfma_f32_16x16x32_bf16 v[100:103], v[44:47], v[84:87], v[100:103]\n\t" \
    "ds_read_b64_tr_b16 v[60:61], v30 offset:36864\n\t" \
    "v_mfma_f32_16x16x32_bf16 v[104:107], v[44:47], v[88:91], v[104:107]\n\t" \
    "buffer_load_dwordx4 v38, s[36:39], s60 offen lds\n\t" \
    "v_mfma_f32_16x16x32_bf16 v[108:111], v[44:47], v[92:95], v[108:111]\n\t" \
    "ds_read_b64_tr_b16 v[62:63], v30 offset:39936\n\t" \
    "s_waitcnt lgkmcnt(6)\n\t" \
    "v_mfma_f32_16x16x32_bf16 v[112:115], v[48:51], v[80:83], v[112:115]\n\t" \
    "ds_read_b64_tr_b16 v[44:45], v30 offset:43008\n\t" \
    "v_mfma_f32_16x16x32_bf16 v[116:119], v[48:51], v[84:87], v[116:119]\n\t" \
    "buffer_load_dwordx4 v38, s[36:39], s60 offen offset:1024 lds\n\t" \
    "v_mfma_f32_16x16x32_bf16 v[120:123], v[48:51], v[88:91], v[120:123]\n\t" \
    "ds_read_b64_tr_b16 v[46:47], v30 offset:46080\n\t" \
    "v_mfma_f32_16x16x32_bf16 v[124:127], v[48:51], v[92:95], v[124:127]\n\t" \
    "s_waitcnt lgkmcnt(6)\n\t" \
    "v_mfma_f32_16x16x32_bf16 v[128:131], v[52:55], v[80:83], v[128:131]\n\t" \
    "s_add_u32 m0, s55, 0\n\t" \
    "v_mfma_f32_16x16x32_bf16 v[132:135], v[52:55], v[84:87], v[132:135]\n\t" \
    "ds_read_b64_tr_b16 v[48:49], v31 offset:36896\n\t" \
    "v_mfma_f32_16x16x32_bf16 v[136:139], v[52:55], v[88:91], v[136:139]\n\t" \
    "buffer_load_dwordx4 v35, s[40:43], s61 offen lds\n\t" \
    "v_mfma_f32_16x16x32_bf16 v[140:143], v[52:55], v[92:95], v[140:143]\n\t" \
    "ds_read_b64_tr_b16 v[50:51], v31 offset:39968\n\t" \
    "s_waitcnt lgkmcnt(6)\n\t" \
    "v_mfma_f32_16x16x32_bf16 v[144:147], v[56:59], v[80:83], v[144:147]\n\t" \
    "s_add_u32 m0, s56, 0\n\t" \
    "v_mfma_f32_16x16x32_bf16 v[148:151], v[56:59], v[84:87], v[148:151]\n\t" \
    "ds_read_b64_tr_b16 v[52:53], v31 offset:43040\n\t" \
    "v_mfma_f32_16x16x32_bf16 v[152:155], v[56:59], v[88:91], v[152:155]\n\t" \
    "buffer_load_dwordx4 v36, s[44:47], s62 offen lds\n\t" \
    "v_mfma_f32_16x16x32_bf16 v[156:159], v[56:59], v[92:95], v[156:159]\n\t" \
    "ds_read_b64_tr_b16 v[54:55], v31 offset:46112\n\t" \
    "s_waitcnt lgkmcnt(6)\n\t" \
    "v_mfma_f32_16x16x32_bf16 v[160:163], v[60:63], v[80:83], v[160:163]\n\t" \
    "s_add_u32 m0, s57, 0\n\t" \
    "v_mfma_f32_16x16x32_bf16 v[164:167], v[60:63], v[84:87], v[164:167]\n\t" \
    "ds_read_b64_tr_b16 v[56:57], v30 offset:36928\n\t" \
    "v_mfma_f32_16x16x32_bf16 v[168:171], v[60:63], v[88:91], v[168:171]\n\t" \
    "buffer_load_dwordx4 v37, s[48:51], s63 offen lds\n\t" \
    "v_mfma_f32_16x16x32_bf16 v[172:175], v[60:63], v[92:95], v[172:175]\n\t" \
    "ds_read_b64_tr_b16 v[58:59], v30 offset:40000\n\t" \
    "s_waitcnt lgkmcnt(6)\n\t" \
    "v_mfma_f32_16x16x32_bf16 v[176:179], v[44:47], v[80:83], v[176:179]\n\t" \
    "ds_read_b64_tr_b16 v[60:61], v30 offset:43072\n\t" \
    "ds_read_b64_tr_b16 v[62:63], v30 offset:46144\n\t" \
    "v_mfma_f32_16x16x32_bf16 v[180:183], v[44:47], v[84:87], v[180:183]\n\t" \
    "s_add_u32 s60, s60, 131072\n\t" \
    "v_mfma_f32_16x16x32_bf16 v[184:187], v[44:47], v[88:91], v[184:187]\n\t" \
    "s_add_u32 s61, s61, s52\n\t" \
    "s_add_u32 s62, s62, s53\n\t" \
    "v_mfma_f32_16x16x32_bf16 v[188:191], v[44:47], v[92:95], v[188:191]\n\t" \
    "s_add_u32 s63, s63, s54\n\t" \
    "s_waitcnt lgkmcnt(6)\n\t" \
    "v_mfma_f32_16x16x32_bf16 v[192:195], v[48:51], v[80:83], v[192:195]\n\t" \
    "v_add_u32 v28, 0x12000, v28\n\t" \
    "v_add_u32 v29, 0x12000, v29\n\t" \
    "v_mfma_f32_16x16x32_bf16 v[196:199], v[48:51], v[84:87], v[196:199]\n\t" \
    "v_add_u32 v30, 0x12000, v30\n\t" \
    "v_mfma_f32_16x16x32_bf16 v[200:203], v[48:51], v[88:91], v[200:203]\n\t" \
    "v_add_u32 v31, 0x12000, v31\n\t" \
    "v_mfma_f32_16x16x32_bf16 v[204:207], v[48:51], v[92:95], v[204:207]\n\t" \
    "v_add_u32 v34, 0x12000, v34\n\t" \
    "s_waitcnt vmcnt(10) lgkmcnt(0)\n\t" \
    "s_barrier\n\t" \
    "v_mfma_f32_16x16x32_bf16 v[208:211], v[52:55], v[80:83], v[208:211]\n\t" \
    "ds_read_b128 v[64:67], v34 offset:0\n\t" \
    "ds_read_b64_tr_b16 v[44:45], v28 offset:0\n\t" \
    "v_mfma_f32_16x16x32_bf16 v[212:215], v[52:55], v[84:87], v[212:215]\n\t" \
    "ds_read_b64_tr_b16 v[46:47], v28 offset:2048\n\t" \
    "v_mfma_f32_16x16x32_bf16 v[216:219], v[52:55], v[88:91], v[216:219]\n\t" \
    "ds_read_b128 v[68:71], v34 offset:1024\n\t" \
    "ds_read_b128 v[72:75], v34 offset:2048\n\t" \
    "v_mfma_f32_16x16x32_bf16 v[220:223], v[52:55], v[92:95], v[220:223]\n\t" \
    "ds_read_b128 v[76:79], v34 offset:3072\n\t" \
    "v_mfma_f32_16x16x32_bf16 v[224:227], v[56:59], v[80:83], v[224:227]\n\t" \
    "ds_read_b64_tr_b16 v[48:49], v29 offset:0\n\t" \
    "v_mfma_f32_16x16x32_bf16 v[228:231], v[56:59], v[84:87], v[228:231]\n\t" \
    "ds_read_b64_tr_b16 v[50:51], v29 offset:2048\n\t" \
    "v_mfma_f32_16x16x32_bf16 v[232:235], v[56:59], v[88:91], v[232:235]\n\t" \
    "ds_read_b64_tr_b16 v[52:53], v28 offset:4096\n\t" \
    "v_mfma_f32_16x16x32_bf16 v[236:239], v[56:59], v[92:95], v[236:239]\n\t" \
    "ds_read_b64_tr_b16 v[54:55], v28 offset:6144\n\t" \
    "v_mfma_f32_16x16x32_bf16 v[240:243], v[60:63], v[80:83], v[240:243]\n\t" \
    "ds_read_b64_tr_b16 v[56:57], v29 offset:4096\n\t" \
    "v_mfma_f32_16x16x32_bf16 v[244:247], v[60:63], v[84:87], v[244:247]\n\t" \
    "v_mfma_f32_16x16x32_bf16 v[248:251], v[60:63], v[88:91], v[248:251]\n\t" \
    "ds_read_b64_tr_b16 v[58:59], v29 offset:6144\n\t" \
    "v_mfma_f32_16x16x32_bf16 v[252:255], v[60:63], v[92:95], v[252:255]\n\t" \
    "s_waitcnt lgkmcnt(6)\n\t" \
    "v_mfma_f32_16x16x32_bf16 v[96:99], v[44:47], v[64:67], v[96:99]\n\t" \
    "s_add_u32 m0, s58, 36864\n\t" \
    "v_mfma_f32_16x16x32_bf16 v[100:103], v[44:47], v[68:71], v[100:103]\n\t" \
    "ds_read_b64_tr_b16 v[60:61], v30 offset:0\n\t" \
    "v_mfma_f32_16x16x32_bf16 v[104:107], v[44:47], v[72:75], v[104:107]\n\t" \
    "buffer_load_dwordx4 v38, s[36:39], s60 offen lds\n\t" \
    "v_mfma_f32_16x16x32_bf16 v[108:111], v[44:47], v[76:79], v[108:111]\n\t" \
    "ds_read_b64_tr_b16 v[62:63], v30 offset:3072\n\t" \
    "s_waitcnt lgkmcnt(6)\n\t" \
    "v_mfma_f32_16x16x32_bf16 v[112:115], v[48:51], v[64:67], v[112:115]\n\t" \
    "ds_read_b64_tr_b16 v[44:45], v30 offset:6144\n\t" \
    "v_mfma_f32_16x16x32_bf16 v[116:119], v[48:51], v[68:71], v[116:119]\n\t" \
    "buffer_load_dwordx4 v38, s[36:39], s60 offen offset:1024 lds\n\t" \
    "v_mfma_f32_16x16x32_bf16 v[120:123], v[48:51], v[72:75], v[120:123]\n\t" \
    "ds_read_b64_tr_b16 v[46:47], v30 offset:9216\n\t" \
    "v_mfma_f32_16x16x32_bf16 v[124:127], v[48:51], v[76:79], v[124:127]\n\t" \
    "s_waitcnt lgkmcnt(6)\n\t" \
    "v_mfma_f32_16x16x32_bf16 v[128:131], v[52:55], v[64:67], v[128:131]\n\t" \
    "s_add_u32 m0, s55, 36864\n\t" \
    "v_mfma_f32_16x16x32_bf16 v[132:135], v[52:55], v[68:71], v[132:135]\n\t" \
    "ds_read_b64_tr_b16 v[48:49], v31 offset:32\n\t" \
    "v_mfma_f32_16x16x32_bf16 v[136:139], v[52:55], v[72:75], v[136:139]\n\t" \
    "buffer_load_dwordx4 v35, s[40:43], s61 offen lds\n\t" \
    "v_mfma_f32_16x16x32_bf16 v[140:143], v[52:55], v[76:79], v[140:143]\n\t" \
    "ds_read_b64_tr_b16 v[50:51], v31 offset:3104\n\t" \
    "s_waitcnt lgkmcnt(6)\n\t" \
    "v_mfma_f32_16x16x32_bf16 v[144:147], v[56:59], v[64:67], v[144:147]\n\t" \
    "s_add_u32 m0, s56, 36864\n\t" \
    "v_mfma_f32_16x16x32_bf16 v[148:151], v[56:59], v[68:71], v[148:151]\n\t" \
    "ds_read_b64_tr_b16 v[52:53], v31 offset:6176\n\t" \
    "v_mfma_f32_16x16x32_bf16 v[152:155], v[56:59], v[72:75], v[152:155]\n\t" \
    "buffer_load_dwordx4 v36, s[44:47], s62 offen lds\n\t" \
    "v_mfma_f32_16x16x32_bf16 v[156:159], v[56:59], v[76:79], v[156:159]\n\t" \
    "ds_read_b64_tr_b16 v[54:55], v31 offset:9248\n\t" \
    "s_waitcnt lgkmcnt(6)\n\t" \
    "v_mfma_f32_16x16x32_bf16 v[160:163], v[60:63], v[64:67], v[160:163]\n\t" \
    "s_add_u32 m0, s57, 36864\n\t" \
    "v_mfma_f32_16x16x32_bf16 v[164:167], v[60:63], v[68:71], v[164:167]\n\t" \
    "ds_read_b64_tr_b16 v[56:57], v30 offset:64\n\t" \
    "v_mfma_f32_16x16x32_bf16 v[168:171], v[60:63], v[72:75], v[168:171]\n\t" \
    "buffer_load_dwordx4 v37, s[48:51], s63 offen lds\n\t" \
    "v_mfma_f32_16x16x32_bf16 v[172:175], v[60:63], v[76:79], v[172:175]\n\t" \
    "ds_read_b64_tr_b16 v[58:59], v30 offset:3136\n\t" \
    "s_waitcnt lgkmcnt(6)\n\t" \
    "v_mfma_f32_16x16x32_bf16 v[176:179], v[44:47], v[64:67], v[176:179]\n\t" \
    "ds_read_b64_tr_b16 v[60:61], v30 offset:6208\n\t" \
    "ds_read_b64_tr_b16 v[62:63], v30 offset:9280\n\t" \
    "v_mfma_f32_16x16x32_bf16 v[180:183], v[44:47], v[68:71], v[180:183]\n\t" \
    "s_add_u32 s60, s60, 131072\n\t" \
    "v_mfma_f32_16x16x32_bf16 v[184:187], v[44:47], v[72:75], v[184:187]\n\t" \
    "s_add_u32 s61, s61, s52\n\t" \
    "s_add_u32 s62, s62, s53\n\t" \
    "v_mfma_f32_16x16x32_bf16 v[188:191], v[44:47], v[76:79], v[188:191]\n\t" \
    "s_add_u32 s63, s63, s54\n\t" \
    "s_waitcnt lgkmcnt(6)\n\t" \
    "v_mfma_f32_16x16x32_bf16 v[192:195], v[48:51], v[64:67], v[192:195]\n\t" \
    "v_mfma_f32_16x16x32_bf16 v[196:199], v[48:51], v[68:71], v[196:199]\n\t" \
    "v_mfma_f32_16x16x32_bf16 v[200:203], v[48:51], v[72:75], v[200:203]\n\t" \
    "v_mfma_f32_16x16x32_bf16 v[204:207], v[48:51], v[76:79], v[204:207]\n\t" \
    "s_waitcnt vmcnt(10) lgkmcnt(0)\n\t" \
    "s_barrier\n\t" \
    "v_mfma_f32_16x16x32_bf16 v[208:211], v[52:55], v[64:67], v[208:211]\n\t" \
    "ds_read_b128 v[80:83], v34 offset:36864\n\t" \
    "ds_read_b64_tr_b16 v[44:45], v28 offset:36864\n\t" \
    "v_mfma_f32_16x16x32_bf16 v[212:215], v[52:55], v[68:71], v[212:215]\n\t" \
    "ds_read_b64_tr_b16 v[46:47], v28 offset:38912\n\t" \
    "v_mfma_f32_16x16x32_bf16 v[216:219], v[52:55], v[72:75], v[216:219]\n\t" \
    "ds_read_b128 v[84:87], v34 offset:37888\n\t" \
    "ds_read_b128 v[88:91], v34 offset:38912\n\t" \
    "v_mfma_f32_16x16x32_bf16 v[220:223], v[52:55], v[76:79], v[220:223]\n\t" \
    "ds_read_b128 v[92:95], v34 offset:39936\n\t" \
    "v_mfma_f32_16x16x32_bf16 v[224:227], v[56:59], v[64:67], v[224:227]\n\t" \
    "ds_read_b64_tr_b16 v[48:49], v29 offset:36864\n\t" \
    "v_mfma_f32_16x16x32_bf16 v[228:231], v[56:59], v[68:71], v[228:231]\n\t" \
    "ds_read_b64_tr_b16 v[50:51], v29 offset:38912\n\t" \
    "v_mfma_f32_16x16x32_bf16 v[232:235], v[56:59], v[72:75], v[232:235]\n\t" \
    "ds_read_b64_tr_b16 v[52:53], v28 offset:40960\n\t" \
    "v_mfma_f32_16x16x32_bf16 v[236:239], v[56:59], v[76:79], v[236:239]\n\t" \
    "ds_read_b64_tr_b16 v[54:55], v28 offset:43008\n\t" \
    "v_mfma_f32_16x16x32_bf16 v[240:243], v[60:63], v[64:67], v[240:243]\n\t" \
    "ds_read_b64_tr_b16 v[56:57], v29 offset:40960\n\t" \
    "v_mfma_f32_16x16x32_bf16 v[244:247], v[60:63], v[68:71], v[244:247]\n\t" \
    "v_mfma_f32_16x16x32_bf16 v[248:251], v[60:63], v[72:75], v[248:251]\n\t" \
    "ds_read_b64_tr_b16 v[58:59], v29 offset:43008\n\t" \
    "v_mfma_f32_16x16x32_bf16 v[252:255], v[60:63], v[76:79], v[252:255]\n\t" \
    "s_cmp_eq_u32 s64, 1\n\t" \
    "s_cbranch_scc0 LK2PF3_v0_%=\n\t" \
    "s_sub_u32 s55, s55, 73728\n\t" \
    "s_sub_u32 s56, s56, 73728\n\t" \
    "s_sub_u32 s57, s57, 73728\n\t" \
    "s_sub_u32 s58, s58, 73728\n\t" \
    "LK2PF3_v0_%=:\n\t" \
    "s_waitcnt lgkmcnt(6)\n\t" \
    "v_mfma_f32_16x16x32_bf16 v[96:99], v[44:47], v[80:83], v[96:99]\n\t" \
    "s_add_u32 m0, s58, 73728\n\t" \
    "v_mfma_f32_16x16x32_bf16 v[100:103], v[44:47], v[84:87], v[100:103]\n\t" \
    "ds_read_b64_tr_b16 v[60:61], v30 offset:36864\n\t" \
    "v_mfma_f32_16x16x32_bf16 v[104:107], v[44:47], v[88:91], v[104:107]\n\t" \
    "buffer_load_dwordx4 v38, s[36:39], s60 offen lds\n\t" \
    "v_mfma_f32_16x16x32_bf16 v[108:111], v[44:47], v[92:95], v[108:111]\n\t" \
    "ds_read_b64_tr_b16 v[62:63], v30 offset:39936\n\t" \
    "s_waitcnt lgkmcnt(6)\n\t" \
    "v_mfma_f32_16x16x32_bf16 v[112:115], v[48:51], v[80:83], v[112:115]\n\t" \
    "ds_read_b64_tr_b16 v[44:45], v30 offset:43008\n\t" \
    "v_mfma_f32_16x16x32_bf16 v[116:119], v[48:51], v[84:87], v[116:119]\n\t" \
    "buffer_load_dwordx4 v38, s[36:39], s60 offen offset:1024 lds\n\t" \
    "v_mfma_f32_16x16x32_bf16 v[120:123], v[48:51], v[88:91], v[120:123]\n\t" \
    "ds_read_b64_tr_b16 v[46:47], v30 offset:46080\n\t" \
    "v_mfma_f32_16x16x32_bf16 v[124:127], v[48:51], v[92:95], v[124:127]\n\t" \
    "s_waitcnt lgkmcnt(6)\n\t" \
    "v_mfma_f32_16x16x32_bf16 v[128:131], v[52:55], v[80:83], v[128:131]\n\t" \
    "s_add_u32 m0, s55, 73728\n\t" \
    "v_mfma_f32_16x16x32_bf16 v[132:135], v[52:55], v[84:87], v[132:135]\n\t" \
    "ds_read_b64_tr_b16 v[48:49], v31 offset:36896\n\t" \
    "v_mfma_f32_16x16x32_bf16 v[136:139], v[52:55], v[88:91], v[136:139]\n\t" \
    "buffer_load_dwordx4 v35, s[40:43], s61 offen lds\n\t" \
    "v_mfma_f32_16x16x32_bf16 v[140:143], v[52:55], v[92:95], v[140:143]\n\t" \
    "ds_read_b64_tr_b16 v[50:51], v31 offset:39968\n\t" \
    "s_waitcnt lgkmcnt(6)\n\t" \
    "v_mfma_f32_16x16x32_bf16 v[144:147], v[56:59], v[80:83], v[144:147]\n\t" \
    "s_add_u32 m0, s56, 73728\n\t" \
    "v_mfma_f32_16x16x32_bf16 v[148:151], v[56:59], v[84:87], v[148:151]\n\t" \
    "ds_read_b64_tr_b16 v[52:53], v31 offset:43040\n\t" \
    "v_mfma_f32_16x16x32_bf16 v[152:155], v[56:59], v[88:91], v[152:155]\n\t" \
    "buffer_load_dwordx4 v36, s[44:47], s62 offen lds\n\t" \
    "v_mfma_f32_16x16x32_bf16 v[156:159], v[56:59], v[92:95], v[156:159]\n\t" \
    "ds_read_b64_tr_b16 v[54:55], v31 offset:46112\n\t" \
    "s_waitcnt lgkmcnt(6)\n\t" \
    "v_mfma_f32_16x16x32_bf16 v[160:163], v[60:63], v[80:83], v[160:163]\n\t" \
    "s_add_u32 m0, s57, 73728\n\t" \
    "v_mfma_f32_16x16x32_bf16 v[164:167], v[60:63], v[84:87], v[164:167]\n\t" \
    "ds_read_b64_tr_b16 v[56:57], v30 offset:36928\n\t" \
    "v_mfma_f32_16x16x32_bf16 v[168:171], v[60:63], v[88:91], v[168:171]\n\t" \
    "buffer_load_dwordx4 v37, s[48:51], s63 offen lds\n\t" \
    "v_mfma_f32_16x16x32_bf16 v[172:175], v[60:63], v[92:95], v[172:175]\n\t" \
    "ds_read_b64_tr_b16 v[58:59], v30 offset:40000\n\t" \
    "s_waitcnt lgkmcnt(6)\n\t" \
    "v_mfma_f32_16x16x32_bf16 v[176:179], v[44:47], v[80:83], v[176:179]\n\t" \
    "ds_read_b64_tr_b16 v[60:61], v30 offset:43072\n\t" \
    "ds_read_b64_tr_b16 v[62:63], v30 offset:46144\n\t" \
    "v_mfma_f32_16x16x32_bf16 v[180:183], v[44:47], v[84:87], v[180:183]\n\t" \
    "s_add_u32 s60, s60, 131072\n\t" \
    "v_mfma_f32_16x16x32_bf16 v[184:187], v[44:47], v[88:91], v[184:187]\n\t" \
    "s_add_u32 s61, s61, s52\n\t" \
    "s_add_u32 s62, s62, s53\n\t" \
    "v_mfma_f32_16x16x32_bf16 v[188:191], v[44:47], v[92:95], v[188:191]\n\t" \
    "s_add_u32 s63, s63, s54\n\t" \
    "s_waitcnt lgkmcnt(6)\n\t" \
    "v_mfma_f32_16x16x32_bf16 v[192:195], v[48:51], v[80:83], v[192:195]\n\t" \
    "v_subrev_u32 v28, 0x12000, v28\n\t" \
    "v_subrev_u32 v29, 0x12000, v29\n\t" \
    "v_mfma_f32_16x16x32_bf16 v[196:199], v[48:51], v[84:87], v[196:199]\n\t" \
    "v_subrev_u32 v30, 0x12000, v30\n\t" \
    "v_mfma_f32_16x16x32_bf16 v[200:203], v[48:51], v[88:91], v[200:203]\n\t" \
    "v_subrev_u32 v31, 0x12000, v31\n\t" \
    "v_mfma_f32_16x16x32_bf16 v[204:207], v[48:51], v[92:95], v[204:207]\n\t" \
    "v_subrev_u32 v34, 0x12000, v34\n\t" \
    "s_waitcnt vmcnt(10) lgkmcnt(0)\n\t" \
    "s_barrier\n\t" \
    "v_mfma_f32_16x16x32_bf16 v[208:211], v[52:55], v[80:83], v[208:211]\n\t" \
    "ds_read_b128 v[64:67], v34 offset:0\n\t" \
    "ds_read_b64_tr_b16 v[44:45], v28 offset:0\n\t" \
    "v_mfma_f32_16x16x32_bf16 v[212:215], v[52:55], v[84:87], v[212:215]\n\t" \
    "ds_read_b64_tr_b16 v[46:47], v28 offset:2048\n\t" \
    "v_mfma_f32_16x16x32_bf16 v[216:219], v[52:55], v[88:91], v[216:219]\n\t" \
    "ds_read_b128 v[68:71], v34 offset:1024\n\t" \
    "ds_read_b128 v[72:75], v34 offset:2048\n\t" \
    "v_mfma_f32_16x16x32_bf16 v[220:223], v[52:55], v[92:95], v[220:223]\n\t" \
    "ds_read_b128 v[76:79], v34 offset:3072\n\t" \
    "v_mfma_f32_16x16x32_bf16 v[224:227], v[56:59], v[80:83], v[224:227]\n\t" \
    "ds_read_b64_tr_b16 v[48:49], v29 offset:0\n\t" \
    "v_mfma_f32_16x16x32_bf16 v[228:231], v[56:59], v[84:87], v[228:231]\n\t" \
    "ds_read_b64_tr_b16 v[50:51], v29 offset:2048\n\t" \
    "v_mfma_f32_16x16x32_bf16 v[232:235], v[56:59], v[88:91], v[232:235]\n\t" \
    "ds_read_b64_tr_b16 v[52:53], v28 offset:4096\n\t" \
    "v_mfma_f32_16x16x32_bf16 v[236:239], v[56:59], v[92:95], v[236:239]\n\t" \
    "ds_read_b64_tr_b16 v[54:55], v28 offset:6144\n\t" \
    "v_mfma_f32_16x16x32_bf16 v[240:243], v[60:63], v[80:83], v[240:243]\n\t" \
    "ds_read_b64_tr_b16 v[56:57], v29 offset:4096\n\t" \
    "v_mfma_f32_16x16x32_bf16 v[244:247], v[60:63], v[84:87], v[244:247]\n\t" \
    "v_mfma_f32_16x16x32_bf16 v[248:251], v[60:63], v[88:91], v[248:251]\n\t" \
    "ds_read_b64_tr_b16 v[58:59], v29 offset:6144\n\t" \
    "v_mfma_f32_16x16x32_bf16 v[252:255], v[60:63], v[92:95], v[252:255]\n\t" \
    "s_sub_u32 s64, s64, 1\n\t" \
    "s_cmp_lg_u32 s64, 0\n\t" \
    "s_cbranch_scc1 LK2_v0_%=\n\t" \
    "s_branch LK2END_%=\n\t" \
    "LK2V1_%=:\n\t" \
    "s_nop 4\n\t" \
    "s_mov_b32 s60, 0\n\t" \
    "s_mov_b32 s61, 0\n\t" \
    "s_mov_b32 s62, 0\n\t" \
    "s_mul_i32 s63, s54, 1\n\t" \
    "s_add_u32 m0, s58, 0\n\t" \
    "s_nop 0\n\t" \
    "buffer_load_dwordx4 v38, s[36:39], s60 offen lds\n\t" \
    "v_mov_b32 v96, 0\n\t" \
    "v_mov_b32 v97, 0\n\t" \
    "v_mov_b32 v98, 0\n\t" \
    "v_mov_b32 v99, 0\n\t" \
    "v_mov_b32 v100, 0\n\t" \
    "v_mov_b32 v101, 0\n\t" \
    "v_mov_b32 v102, 0\n\t" \
    "v_mov_b32 v103, 0\n\t" \
    "v_mov_b32 v104, 0\n\t" \
    "v_mov_b32 v105, 0\n\t" \
    "v_mov_b32 v106, 0\n\t" \
    "v_mov_b32 v107, 0\n\t" \
    "v_mov_b32 v108, 0\n\t" \
    "v_mov_b32 v109, 0\n\t" \
    "v_mov_b32 v110, 0\n\t" \
    "v_mov_b32 v111, 0\n\t" \
    "buffer_load_dwordx4 v38, s[36:39], s60 offen offset:1024 lds\n\t" \
    "v_mov_b32 v112, 0\n\t" \
    "v_mov_b32 v113, 0\n\t" \
    "v_mov_b32 v114, 0\n\t" \
    "v_mov_b32 v115, 0\n\t" \
    "v_mov_b32 v116, 0\n\t" \
    "v_mov_b32 v117, 0\n\t" \
    "v_mov_b32 v118, 0\n\t" \
    "v_mov_b32 v119, 0\n\t" \
    "v_mov_b32 v120, 0\n\t" \
    "v_mov_b32 v121, 0\n\t" \
    "v_mov_b32 v122, 0\n\t" \
    "v_mov_b32 v123, 0\n\t" \
    "v_mov_b32 v124, 0\n\t" \
    "v_mov_b32 v125, 0\n\t" \
    "v_mov_b32 v126, 0\n\t" \
    "v_mov_b32 v127, 0\n\t" \
    "s_add_u32 m0, s55, 0\n\t" \
    "s_nop 0\n\t" \
    "buffer_load_dwordx4 v35, s[40:43], s61 offen lds\n\t" \
    "v_mov_b32 v128, 0\n\t" \
    "v_mov_b32 v129, 0\n\t" \
    "v_mov_b32 v130, 0\n\t" \
    "v_mov_b32 v131, 0\n\t" \
    "v_mov_b32 v132, 0\n\t" \
    "v_mov_b32 v133, 0\n\t" \
    "v_mov_b32 v134, 0\n\t" \
    "v_mov_b32 v135, 0\n\t" \
    "v_mov_b32 v136, 0\n\t" \
    "v_mov_b32 v137, 0\n\t" \
    "v_mov_b32 v138, 0\n\t" \
    "v_mov_b32 v139, 0\n\t" \
    "v_mov_b32 v140, 0\n\t" \
    "v_mov_b32 v141, 0\n\t" \
    "v_mov_b32 v142, 0\n\t" \
    "v_mov_b32 v143, 0\n\t" \
    "s_add_u32 m0, s56, 0\n\t" \
    "s_nop 0\n\t" \
    "buffer_load_dwordx4 v36, s[44:47], s62 offen lds\n\t" \
    "v_mov_b32 v144, 0\n\t" \
    "v_mov_b32 v145, 0\n\t" \
    "v_mov_b32 v146, 0\n\t" \
    "v_mov_b32 v147, 0\n\t" \
    "v_mov_b32 v148, 0\n\t" \
    "v_mov_b32 v149, 0\n\t" \
    "v_mov_b32 v150, 0\n\t" \
    "v_mov_b32 v151, 0\n\t" \
    "v_mov_b32 v152, 0\n\t" \
    "v_mov_b32 v153, 0\n\t" \
    "v_mov_b32 v154, 0\n\t" \
    "v_mov_b32 v155, 0\n\t" \
    "v_mov_b32 v156, 0\n\t" \
    "v_mov_b32 v157, 0\n\t" \
    "v_mov_b32 v158, 0\n\t" \
    "v_mov_b32 v159, 0\n\t" \
    "s_add_u32 m0, s57, 0\n\t" \
    "s_nop 0\n\t" \
    "buffer_load_dwordx4 v37, s[48:51], s63 offen lds\n\t" \
    "v_mov_b32 v160, 0\n\t" \
    "v_mov_b32 v161, 0\n\t" \
    "v_mov_b32 v162, 0\n\t" \
    "v_mov_b32 v163, 0\n\t" \
    "v_mov_b32 v164, 0\n\t" \
    "v_mov_b32 v165, 0\n\t" \
    "v_mov_b32 v166, 0\n\t" \
    "v_mov_b32 v167, 0\n\t" \
    "v_mov_b32 v168, 0\n\t" \
    "v_mov_b32 v169, 0\n\t" \
    "v_mov_b32 v170, 0\n\t" \
    "v_mov_b32 v171, 0\n\t" \
    "v_mov_b32 v172, 0\n\t" \
    "v_mov_b32 v173, 0\n\t" \
    "v_mov_b32 v174, 0\n\t" \
    "v_mov_b32 v175, 0\n\t" \
    "s_add_u32 s60, s60, 131072\n\t" \
    "s_add_u32 s61, s61, s52\n\t" \
    "s_add_u32 s62, s62, s53\n\t" \
    "s_add_u32 s63, s63, s54\n\t" \
    "s_add_u32 m0, s58, 36864\n\t" \
    "s_nop 0\n\t" \
    "buffer_load_dwordx4 v38, s[36:39], s60 offen lds\n\t" \
    "v_mov_b32 v176, 0\n\t" \
    "v_mov_b32 v177, 0\n\t" \
    "v_mov_b32 v178, 0\n\t" \
    "v_mov_b32 v179, 0\n\t" \
    "v_mov_b32 v180, 0\n\t" \
    "v_mov_b32 v181, 0\n\t" \
    "v_mov_b32 v182, 0\n\t" \
    "v_mov_b32 v183, 0\n\t" \
    "v_mov_b32 v184, 0\n\t" \
    "v_mov_b32 v185, 0\n\t" \
    "v_mov_b32 v186, 0\n\t" \
    "v_mov_b32 v187, 0\n\t" \
    "v_mov_b32 v188, 0\n\t" \
    "v_mov_b32 v189, 0\n\t" \
    "v_mov_b32 v190, 0\n\t" \
    "v_mov_b32 v191, 0\n\t" \
    "buffer_load_dwordx4 v38, s[36:39], s60 offen offset:1024 lds\n\t" \
    "v_mov_b32 v192, 0\n\t" \
    "v_mov_b32 v193, 0\n\t" \
    "v_mov_b32 v194, 0\n\t" \
    "v_mov_b32 v195, 0\n\t" \
    "v_mov_b32 v196, 0\n\t" \
    "v_mov_b32 v197, 0\n\t" \
    "v_mov_b32 v198, 0\n\t" \
    "v_mov_b32 v199, 0\n\t" \
    "v_mov_b32 v200, 0\n\t" \
    "v_mov_b32 v201, 0\n\t" \
    "v_mov_b32 v202, 0\n\t" \
    "v_mov_b32 v203, 0\n\t" \
    "v_mov_b32 v204, 0\n\t" \
    "v_mov_b32 v205, 0\n\t" \
    "v_mov_b32 v206, 0\n\t" \
    "v_mov_b32 v207, 0\n\t" \
    "s_add_u32 m0, s55, 36864\n\t" \
    "s_nop 0\n\t" \
    "buffer_load_dwordx4 v35, s[40:43], s61 offen lds\n\t" \
    "v_mov_b32 v208, 0\n\t" \
    "v_mov_b32 v209, 0\n\t" \
    "v_mov_b32 v210, 0\n\t" \
    "v_mov_b32 v211, 0\n\t" \
    "v_mov_b32 v212, 0\n\t" \
    "v_mov_b32 v213, 0\n\t" \
    "v_mov_b32 v214, 0\n\t" \
    "v_mov_b32 v215, 0\n\t" \
    "v_mov_b32 v216, 0\n\t" \
    "v_mov_b32 v217, 0\n\t" \
    "v_mov_b32 v218, 0\n\t" \
    "v_mov_b32 v219, 0\n\t" \
    "v_mov_b32 v220, 0\n\t" \
    "v_mov_b32 v221, 0\n\t" \
    "v_mov_b32 v222, 0\n\t" \
    "v_mov_b32 v223, 0\n\t" \
    "s_add_u32 m0, s56, 36864\n\t" \
    "s_nop 0\n\t" \
    "buffer_load_dwordx4 v36, s[44:47], s62 offen lds\n\t" \
    "v_mov_b32 v224, 0\n\t" \
    "v_mov_b32 v225, 0\n\t" \
    "v_mov_b32 v226, 0\n\t" \
    "v_mov_b32 v227, 0\n\t" \
    "v_mov_b32 v228, 0\n\t" \
    "v_mov_b32 v229, 0\n\t" \
    "v_mov_b32 v230, 0\n\t" \
    "v_mov_b32 v231, 0\n\t" \
    "v_mov_b32 v232, 0\n\t" \
    "v_mov_b32 v233, 0\n\t" \
    "v_mov_b32 v234, 0\n\t" \
    "v_mov_b32 v235, 0\n\t" \
    "v_mov_b32 v236, 0\n\t" \
    "v_mov_b32 v237, 0\n\t" \
    "v_mov_b32 v238, 0\n\t" \
    "v_mov_b32 v239, 0\n\t" \
    "s_add_u32 m0, s57, 0\n\t" \
    "s_nop 0\n\t" \
    "buffer_load_dwordx4 v37, s[48:51], s63 offen lds\n\t" \
    "v_mov_b32 v240, 0\n\t" \
    "v_mov_b32 v241, 0\n\t" \
    "v_mov_b32 v242, 0\n\t" \
    "v_mov_b32 v243, 0\n\t" \
    "v_mov_b32 v244, 0\n\t" \
    "v_mov_b32 v245, 0\n\t" \
    "v_mov_b32 v246, 0\n\t" \
    "v_mov_b32 v247, 0\n\t" \
    "v_mov_b32 v248, 0\n\t" \
    "v_mov_b32 v249, 0\n\t" \
    "v_mov_b32 v250, 0\n\t" \
    "v_mov_b32 v251, 0\n\t" \
    "v_mov_b32 v252, 0\n\t" \
    "v_mov_b32 v253, 0\n\t" \
    "v_mov_b32 v254, 0\n\t" \
    "v_mov_b32 v255, 0\n\t" \
    "s_add_u32 s60, s60, 131072\n\t" \
    "s_add_u32 s61, s61, s52\n\t" \
    "s_add_u32 s62, s62, s53\n\t" \
    "s_add_u32 s63, s63, s54\n\t" \
    "s_mov_b32 s64, 8\n\t" \
    "s_waitcnt vmcnt(5)\n\t" \
    "s_barrier\n\t" \
    "ds_read_b128 v[64:67], v34 offset:0\n\t" \
    "ds_read_b128 v[68:71], v34 offset:1024\n\t" \
    "ds_read_b128 v[72:75], v34 offset:2048\n\t" \
    "ds_read_b128 v[76:79], v34 offset:3072\n\t" \
    "ds_read_b64_tr_b16 v[44:45], v28 offset:0\n\t" \
    "ds_read_b64_tr_b16 v[46:47], v28 offset:2048\n\t" \
    "ds_read_b64_tr_b16 v[48:49], v29 offset:0\n\t" \
    "ds_read_b64_tr_b16 v[50:51], v29 offset:2048\n\t" \
    "ds_read_b64_tr_b16 v[52:53], v28 offset:4096\n\t" \
    "ds_read_b64_tr_b16 v[54:55], v28 offset:6144\n\t" \
    "ds_read_b64_tr_b16 v[56:57], v29 offset:4096\n\t" \
    "ds_read_b64_tr_b16 v[58:59], v29 offset:6144\n\t" \
    "s_add_u32 m0, s58, 73728\n\t" \
    "s_nop 0\n\t" \
    "buffer_load_dwordx4 v38, s[36:39], s60 offen lds\n\t" \
    "buffer_load_dwordx4 v38, s[36:39], s60 offen offset:1024 lds\n\t" \
    "s_add_u32 m0, s55, 73728\n\t" \
    "s_nop 0\n\t" \
    "buffer_load_dwordx4 v35, s[40:43], s61 offen lds\n\t" \
    "s_add_u32 m0, s56, 73728\n\t" \
    "s_nop 0\n\t" \
    "buffer_load_dwordx4 v36, s[44:47], s62 offen lds\n\t" \
    "s_add_u32 m0, s57, 0\n\t" \
    "s_nop 0\n\t" \
    "buffer_load_dwordx4 v37, s[48:51], s63 offen lds\n\t" \
    "s_add_u32 s60, s60, 131072\n\t" \
    "s_add_u32 s61, s61, s52\n\t" \
    "s_add_u32 s62, s62, s53\n\t" \
    "s_add_u32 s63, s63, s54\n\t" \
    "s_cmp_eq_u32 s73, 0\n\t" \
    "s_cbranch_scc1 LK2NP_v1_%=\n\t" \
    "s_add_u32 m0, s72, 155648\n\t" \
    "s_nop 0\n\t" \
    "global_load_lds_dword v[24:25], off\n\t" \
    "LK2NP_v1_%=:\n\t" \
    "LK2_v1_%=:\n\t" \
    "s_waitcnt lgkmcnt(6)\n\t" \
    "v_mfma_f32_16x16x32_bf16 v[96:99], v[44:47], v[64:67], v[96:99]\n\t" \
    "s_add_u32 m0, s58, 110592\n\t" \
    "v_mfma_f32_16x16x32_bf16 v[100:103], v[44:47], v[68:71], v[100:103]\n\t" \
    "ds_read_b64_tr_b16 v[60:61], v31 offset:96\n\t" \
    "v_mfma_f32_16x16x32_bf16 v[104:107], v[44:47], v[72:75], v[104:107]\n\t" \
    "buffer_load_dwordx4 v38, s[36:39], s60 offen lds\n\t" \
    "v_mfma_f32_16x16x32_bf16 v[108:111], v[44:47], v[76:79], v[108:111]\n\t" \
    "ds_read_b64_tr_b16 v[62:63], v31 offset:3168\n\t" \
    "s_waitcnt lgkmcnt(6)\n\t" \
    "v_mfma_f32_16x16x32_bf16 v[112:115], v[48:51], v[64:67], v[112:115]\n\t" \
    "ds_read_b64_tr_b16 v[44:45], v31 offset:6240\n\t" \
    "v_mfma_f32_16x16x32_bf16 v[116:119], v[48:51], v[68:71], v[116:119]\n\t" \
    "buffer_load_dwordx4 v38, s[36:39], s60 offen offset:1024 lds\n\t" \
    "v_mfma_f32_16x16x32_bf16 v[120:123], v[48:51], v[72:75], v[120:123]\n\t" \
    "ds_read_b64_tr_b16 v[46:47], v31 offset:9312\n\t" \
    "v_mfma_f32_16x16x32_bf16 v[124:127], v[48:51], v[76:79], v[124:127]\n\t" \
    "s_waitcnt lgkmcnt(6)\n\t" \
    "v_mfma_f32_16x16x32_bf16 v[128:131], v[52:55], v[64:67], v[128:131]\n\t" \
    "s_add_u32 m0, s55, 110592\n\t" \
    "v_mfma_f32_16x16x32_bf16 v[132:135], v[52:55], v[68:71], v[132:135]\n\t" \
    "ds_read_b64_tr_b16 v[48:49], v30 offset:128\n\t" \
    "v_mfma_f32_16x16x32_bf16 v[136:139], v[52:55], v[72:75], v[136:139]\n\t" \
    "buffer_load_dwordx4 v35, s[40:43], s61 offen lds\n\t" \
    "v_mfma_f32_16x16x32_bf16 v[140:143], v[52:55], v[76:79], v[140:143]\n\t" \
    "ds_read_b64_tr_b16 v[50:51], v30 offset:3200\n\t" \
    "s_waitcnt lgkmcnt(6)\n\t" \
    "v_mfma_f32_16x16x32_bf16 v[144:147], v[56:59], v[64:67], v[144:147]\n\t" \
    "s_add_u32 m0, s56, 110592\n\t" \
    "v_mfma_f32_16x16x32_bf16 v[148:151], v[56:59], v[68:71], v[148:151]\n\t" \
    "ds_read_b64_tr_b16 v[52:53], v30 offset:6272\n\t" \
    "v_mfma_f32_16x16x32_bf16 v[152:155], v[56:59], v[72:75], v[152:155]\n\t" \
    "buffer_load_dwordx4 v36, s[44:47], s62 offen lds\n\t" \
    "v_mfma_f32_16x16x32_bf16 v[156:159], v[56:59], v[76:79], v[156:159]\n\t" \
    "ds_read_b64_tr_b16 v[54:55], v30 offset:9344\n\t" \
    "s_waitcnt lgkmcnt(6)\n\t" \
    "v_mfma_f32_16x16x32_bf16 v[160:163], v[60:63], v[64:67], v[160:163]\n\t" \
    "s_add_u32 m0, s57, 0\n\t" \
    "v_mfma_f32_16x16x32_bf16 v[164:167], v[60:63], v[68:71], v[164:167]\n\t" \
    "ds_read_b64_tr_b16 v[56:57], v31 offset:160\n\t" \
    "v_mfma_f32_16x16x32_bf16 v[168:171], v[60:63], v[72:75], v[168:171]\n\t" \
    "buffer_load_dwordx4 v37, s[48:51], s63 offen lds\n\t" \
    "v_mfma_f32_16x16x32_bf16 v[172:175], v[60:63], v[76:79], v[172:175]\n\t" \
    "ds_read_b64_tr_b16 v[58:59], v31 offset:3232\n\t" \
    "s_waitcnt lgkmcnt(6)\n\t" \
    "v_mfma_f32_16x16x32_bf16 v[176:179], v[44:47], v[64:67], v[176:179]\n\t" \
    "ds_read_b64_tr_b16 v[60:61], v31 offset:6304\n\t" \
    "ds_read_b64_tr_b16 v[62:63], v31 offset:9376\n\t" \
    "v_mfma_f32_16x16x32_bf16 v[180:183], v[44:47], v[68:71], v[180:183]\n\t" \
    "s_add_u32 s60, s60, 131072\n\t" \
    "v_mfma_f32_16x16x32_bf16 v[184:187], v[44:47], v[72:75], v[184:187]\n\t" \
    "s_add_u32 s61, s61, s52\n\t" \
    "s_add_u32 s62, s62, s53\n\t" \
    "v_mfma_f32_16x16x32_bf16 v[188:191], v[44:47], v[76:79], v[188:191]\n\t" \
    "s_add_u32 s63, s63, s54\n\t" \
    "s_waitcnt lgkmcnt(6)\n\t" \
    "v_mfma_f32_16x16x32_bf16 v[192:195], v[48:51], v[64:67], v[192:195]\n\t" \
    "v_mfma_f32_16x16x32_bf16 v[196:199], v[48:51], v[68:71], v[196:199]\n\t" \
    "v_mfma_f32_16x16x32_bf16 v[200:203], v[48:51], v[72:75], v[200:203]\n\t" \
    "v_mfma_f32_16x16x32_bf16 v[204:207], v[48:51], v[76:79], v[204:207]\n\t" \
    "s_waitcnt vmcnt(10) lgkmcnt(0)\n\t" \
    "s_barrier\n\t" \
    "v_mfma_f32_16x16x32_bf16 v[208:211], v[52:55], v[64:67], v[208:211]\n\t" \
    "ds_read_b128 v[80:83], v34 offset:36864\n\t" \
    "ds_read_b64_tr_b16 v[44:45], v28 offset:36864\n\t" \
    "v_mfma_f32_16x16x32_bf16 v[212:215], v[52:55], v[68:71], v[212:215]\n\t" \
    "ds_read_b64_tr_b16 v[46:47], v28 offset:38912\n\t" \
    "v_mfma_f32_16x16x32_bf16 v[216:219], v[52:55], v[72:75], v[216:219]\n\t" \
    "ds_read_b128 v[84:87], v34 offset:37888\n\t" \
    "ds_read_b128 v[88:91], v34 offset:38912\n\t" \
    "v_mfma_f32_16x16x32_bf16 v[220:223], v[52:55], v[76:79], v[220:223]\n\t" \
    "ds_read_b128 v[92:95], v34 offset:39936\n\t" \
    "v_mfma_f32_16x16x32_bf16 v[224:227], v[56:59], v[64:67], v[224:227]\n\t" \
    "ds_read_b64_tr_b16 v[48:49], v29 offset:36864\n\t" \
    "v_mfma_f32_16x16x32_bf16 v[228:231], v[56:59], v[68:71], v[228:231]\n\t" \
    "ds_read_b64_tr_b16 v[50:51], v29 offset:38912\n\t" \
    "v_mfma_f32_16x16x32_bf16 v[232:235], v[56:59], v[72:75], v[232:235]\n\t" \
    "ds_read_b64_tr_b16 v[52:53], v28 offset:40960\n\t" \
    "v_mfma_f32_16x16x32_bf16 v[236:239], v[56:59], v[76:79], v[236:239]\n\t" \
    "ds_read_b64_tr_b16 v[54:55], v28 offset:43008\n\t" \
    "v_mfma_f32_16x16x32_bf16 v[240:243], v[60:63], v[64:67], v[240:243]\n\t" \
    "ds_read_b64_tr_b16 v[56:57], v29 offset:40960\n\t" \
    "v_mfma_f32_16x16x32_bf16 v[244:247], v[60:63], v[68:71], v[244:247]\n\t" \
    "v_mfma_f32_16x16x32_bf16 v[248:251], v[60:63], v[72:75], v[248:251]\n\t" \
    "ds_read_b64_tr_b16 v[58:59], v29 offset:43008\n\t" \
    "v_mfma_f32_16x16x32_bf16 v[252:255], v[60:63], v[76:79], v[252:255]\n\t" \
    "s_cmp_eq_u32 s64, 1\n\t" \
    "s_cbranch_scc0 LK2PF1_v1_%=\n\t" \
    "s_mov_b32 s40, s68\n\t" \
    "s_mov_b32 s41, s69\n\t" \
    "s_mov_b32 s42, 7340032\n\t" \
    "s_mov_b32 s52, 4194304\n\t" \
    "s_mov_b32 s61, 0\n\t" \
    "v_mov_b32 v35, v39\n\t" \
    "s_mov_b32 s55, s72\n\t" \
    "s_mov_b32 s44, s68\n\t" \
    "s_mov_b32 s45, s69\n\t" \
    "s_mov_b32 s46, 7340032\n\t" \
    "s_mov_b32 s53, 4194304\n\t" \
    "s_mov_b32 s62, 2097152\n\t" \
    "v_mov_b32 v36, v39\n\t" \
    "s_mov_b32 s56, s72\n\t" \
    "s_mov_b32 s50, 0\n\t" \
    "s_mov_b32 s58, s72\n\t" \
    "LK2PF1_v1_%=:\n\t" \
    "s_waitcnt lgkmcnt(6)\n\t" \
    "v_mfma_f32_16x16x32_bf16 v[96:99], v[44:47], v[80:83], v[96:99]\n\t" \
    "s_add_u32 m0, s58, 0\n\t" \
    "v_mfma_f32_16x16x32_bf16 v[100:103], v[44:47], v[84:87], v[100:103]\n\t" \
    "ds_read_b64_tr_b16 v[60:61], v31 offset:36960\n\t" \
    "v_mfma_f32_16x16x32_bf16 v[104:107], v[44:47], v[88:91], v[104:107]\n\t" \
    "buffer_load_dwordx4 v38, s[36:39], s60 offen lds\n\t" \
    "v_mfma_f32_16x16x32_bf16 v[108:111], v[44:47], v[92:95], v[108:111]\n\t" \
    "ds_read_b64_tr_b16 v[62:63], v31 offset:40032\n\t" \
    "s_waitcnt lgkmcnt(6)\n\t" \
    "v_mfma_f32_16x16x32_bf16 v[112:115], v[48:51], v[80:83], v[112:115]\n\t" \
    "ds_read_b64_tr_b16 v[44:45], v31 offset:43104\n\t" \
    "v_mfma_f32_16x16x32_bf16 v[116:119], v[48:51], v[84:87], v[116:119]\n\t" \
    "buffer_load_dwordx4 v38, s[36:39], s60 offen offset:1024 lds\n\t" \
    "v_mfma_f32_16x16x32_bf16 v[120:123], v[48:51], v[88:91], v[120:123]\n\t" \
    "ds_read_b64_tr_b16 v[46:47], v31 offset:46176\n\t" \
    "v_mfma_f32_16x16x32_bf16 v[124:127], v[48:51], v[92:95], v[124:127]\n\t" \
    "s_waitcnt lgkmcnt(6)\n\t" \
    "v_mfma_f32_16x16x32_bf16 v[128:131], v[52:55], v[80:83], v[128:131]\n\t" \
    "s_add_u32 m0, s55, 0\n\t" \
    "v_mfma_f32_16x16x32_bf16 v[132:135], v[52:55], v[84:87], v[132:135]\n\t" \
    "ds_read_b64_tr_b16 v[48:49], v30 offset:36992\n\t" \
    "v_mfma_f32_16x16x32_bf16 v[136:139], v[52:55], v[88:91], v[136:139]\n\t" \
    "buffer_load_dwordx4 v35, s[40:43], s61 offen lds\n\t" \
    "v_mfma_f32_16x16x32_bf16 v[140:143], v[52:55], v[92:95], v[140:143]\n\t" \
    "ds_read_b64_tr_b16 v[50:51], v30 offset:40064\n\t" \
    "s_waitcnt lgkmcnt(6)\n\t" \
    "v_mfma_f32_16x16x32_bf16 v[144:147], v[56:59], v[80:83], v[144:147]\n\t" \
    "s_add_u32 m0, s56, 0\n\t" \
    "v_mfma_f32_16x16x32_bf16 v[148:151], v[56:59], v[84:87], v[148:151]\n\t" \
    "ds_read_b64_tr_b16 v[52:53], v30 offset:43136\n\t" \
    "v_mfma_f32_16x16x32_bf16 v[152:155], v[56:59], v[88:91], v[152:155]\n\t" \
    "buffer_load_dwordx4 v36, s[44:47], s62 offen lds\n\t" \
    "v_mfma_f32_16x16x32_bf16 v[156:159], v[56:59], v[92:95], v[156:159]\n\t" \
    "ds_read_b64_tr_b16 v[54:55], v30 offset:46208\n\t" \
    "s_waitcnt lgkmcnt(6)\n\t" \
    "v_mfma_f32_16x16x32_bf16 v[160:163], v[60:63], v[80:83], v[160:163]\n\t" \
    "s_add_u32 m0, s57, 0\n\t" \
    "v_mfma_f32_16x16x32_bf16 v[164:167], v[60:63], v[84:87], v[164:167]\n\t" \
    "ds_read_b64_tr_b16 v[56:57], v31 offset:37024\n\t" \
    "v_mfma_f32_16x16x32_bf16 v[168:171], v[60:63], v[88:91], v[168:171]\n\t" \
    "buffer_load_dwordx4 v37, s[48:51], s63 offen lds\n\t" \
    "v_mfma_f32_16x16x32_bf16 v[172:175], v[60:63], v[92:95], v[172:175]\n\t" \
    "ds_read_b64_tr_b16 v[58:59], v31 offset:40096\n\t" \
    "s_waitcnt lgkmcnt(6)\n\t" \
    "v_mfma_f32_16x16x32_bf16 v[176:179], v[44:47], v[80:83], v[176:179]\n\t" \
    "ds_read_b64_tr_b16 v[60:61], v31 offset:43168\n\t" \
    "ds_read_b64_tr_b16 v[62:63], v31 offset:46240\n\t" \
    "v_mfma_f32_16x16x32_bf16 v[180:183], v[44:47], v[84:87], v[180:183]\n\t" \
    "s_add_u32 s60, s60, 131072\n\t" \
    "v_mfma_f32_16x16x32_bf16 v[184:187], v[44:47], v[88:91], v[184:187]\n\t" \
    "s_add_u32 s61, s61, s52\n\t" \
    "s_add_u32 s62, s62, s53\n\t" \
    "v_mfma_f32_16x16x32_bf16 v[188:191], v[44:47], v[92:95], v[188:191]\n\t" \
    "s_add_u32 s63, s63, s54\n\t" \
    "s_waitcnt lgkmcnt(6)\n\t" \
    "v_mfma_f32_16x16x32_bf16 v[192:195], v[48:51], v[80:83], v[192:195]\n\t" \
    "v_add_u32 v28, 0x12000, v28\n\t" \
    "v_add_u32 v29, 0x12000, v29\n\t" \
    "v_mfma_f32_16x16x32_bf16 v[196:199], v[48:51], v[84:87], v[196:199]\n\t" \
    "v_add_u32 v30, 0x12000, v30\n\t" \
    "v_mfma_f32_16x16x32_bf16 v[200:203], v[48:51], v[88:91], v[200:203]\n\t" \
    "v_add_u32 v31, 0x12000, v31\n\t" \
    "v_mfma_f32_16x16x32_bf16 v[204:207], v[48:51], v[92:95], v[204:207]\n\t" \
    "v_add_u32 v34, 0x12000, v34\n\t" \
    "s_waitcnt vmcnt(10) lgkmcnt(0)\n\t" \
    "s_barrier\n\t" \
    "v_mfma_f32_16x16x32_bf16 v[208:211], v[52:55], v[80:83], v[208:211]\n\t" \
    "ds_read_b128 v[64:67], v34 offset:0\n\t" \
    "ds_read_b64_tr_b16 v[44:45], v28 offset:0\n\t" \
    "v_mfma_f32_16x16x32_bf16 v[212:215], v[52:55], v[84:87], v[212:215]\n\t" \
    "ds_read_b64_tr_b16 v[46:47], v28 offset:2048\n\t" \
    "v_mfma_f32_16x16x32_bf16 v[216:219], v[52:55], v[88:91], v[216:219]\n\t" \
    "ds_read_b128 v[68:71], v34 offset:1024\n\t" \
    "ds_read_b128 v[72:75], v34 offset:2048\n\t" \
    "v_mfma_f32_16x16x32_bf16 v[220:223], v[52:55], v[92:95], v[220:223]\n\t" \
    "ds_read_b128 v[76:79], v34 offset:3072\n\t" \
    "v_mfma_f32_16x16x32_bf16 v[224:227], v[56:59], v[80:83], v[224:227]\n\t" \
    "ds_read_b64_tr_b16 v[48:49], v29 offset:0\n\t" \
    "v_mfma_f32_16x16x32_bf16 v[228:231], v[56:59], v[84:87], v[228:231]\n\t" \
    "ds_read_b64_tr_b16 v[50:51], v29 offset:2048\n\t" \
    "v_mfma_f32_16x16x32_bf16 v[232:235], v[56:59], v[88:91], v[232:235]\n\t" \
    "ds_read_b64_tr_b16 v[52:53], v28 offset:4096\n\t" \
    "v_mfma_f32_16x16x32_bf16 v[236:239], v[56:59], v[92:95], v[236:239]\n\t" \
    "ds_read_b64_tr_b16 v[54:55], v28 offset:6144\n\t" \
    "v_mfma_f32_16x16x32_bf16 v[240:243], v[60:63], v[80:83], v[240:243]\n\t" \
    "ds_read_b64_tr_b16 v[56:57], v29 offset:4096\n\t" \
    "v_mfma_f32_16x16x32_bf16 v[244:247], v[60:63], v[84:87], v[244:247]\n\t" \
    "v_mfma_f32_16x16x32_bf16 v[248:251], v[60:63], v[88:91], v[248:251]\n\t" \
    "ds_read_b64_tr_b16 v[58:59], v29 offset:6144\n\t" \
    "v_mfma_f32_16x16x32_bf16 v[252:255], v[60:63], v[92:95], v[252:255]\n\t" \
    "s_waitcnt lgkmcnt(6)\n\t" \
    "v_mfma_f32_16x16x32_bf16 v[96:99], v[44:47], v[64:67], v[96:99]\n\t" \
    "s_add_u32 m0, s58, 36864\n\t" \
    "v_mfma_f32_16x16x32_bf16 v[100:103], v[44:47], v[68:71], v[100:103]\n\t" \
    "ds_read_b64_tr_b16 v[60:61], v31 offset:96\n\t" \
    "v_mfma_f32_16x16x32_bf16 v[104:107], v[44:47], v[72:75], v[104:107]\n\t" \
    "buffer_load_dwordx4 v38, s[36:39], s60 offen lds\n\t" \
    "v_mfma_f32_16x16x32_bf16 v[108:111], v[44:47], v[76:79], v[108:111]\n\t" \
    "ds_read_b64_tr_b16 v[62:63], v31 offset:3168\n\t" \
    "s_waitcnt lgkmcnt(6)\n\t" \
    "v_mfma_f32_16x16x32_bf16 v[112:115], v[48:51], v[64:67], v[112:115]\n\t" \
    "ds_read_b64_tr_b16 v[44:45], v31 offset:6240\n\t" \
    "v_mfma_f32_16x16x32_bf16 v[116:119], v[48:51], v[68:71], v[116:119]\n\t" \
    "buffer_load_dwordx4 v38, s[36:39], s60 offen offset:1024 lds\n\t" \
    "v_mfma_f32_16x16x32_bf16 v[120:123], v[48:51], v[72:75], v[120:123]\n\t" \
    "ds_read_b64_tr_b16 v[46:47], v31 offset:9312\n\t" \
    "v_mfma_f32_16x16x32_bf16 v[124:127], v[48:51], v[76:79], v[124:127]\n\t" \
    "s_waitcnt lgkmcnt(6)\n\t" \
    "v_mfma_f32_16x16x32_bf16 v[128:131], v[52:55], v[64:67], v[128:131]\n\t" \
    "s_add_u32 m0, s55, 36864\n\t" \
    "v_mfma_f32_16x16x32_bf16 v[132:135], v[52:55], v[68:71], v[132:135]\n\t" \
    "ds_read_b64_tr_b16 v[48:49], v30 offset:128\n\t" \
    "v_mfma_f32_16x16x32_bf16 v[136:139], v[52:55], v[72:75], v[136:139]\n\t" \
    "buffer_load_dwordx4 v35, s[40:43], s61 offen lds\n\t" \
    "v_mfma_f32_16x16x32_bf16 v[140:143], v[52:55], v[76:79], v[140:143]\n\t" \
    "ds_read_b64_tr_b16 v[50:51], v30 offset:3200\n\t" \
    "s_waitcnt lgkmcnt(6)\n\t" \
    "v_mfma_f32_16x16x32_bf16 v[144:147], v[56:59], v[64:67], v[144:147]\n\t" \
    "s_add_u32 m0, s56, 36864\n\t" \
    "v_mfma_f32_16x16x32_bf16 v[148:151], v[56:59], v[68:71], v[148:151]\n\t" \
    "ds_read_b64_tr_b16 v[52:53], v30 offset:6272\n\t" \
    "v_mfma_f32_16x16x32_bf16 v[152:155], v[56:59], v[72:75], v[152:155]\n\t" \
    "buffer_load_dwordx4 v36, s[44:47], s62 offen lds\n\t" \
    "v_mfma_f32_16x16x32_bf16 v[156:159], v[56:59], v[76:79], v[156:159]\n\t" \
    "ds_read_b64_tr_b16 v[54:55], v30 offset:9344\n\t" \
    "s_waitcnt lgkmcnt(6)\n\t" \
    "v_mfma_f32_16x16x32_bf16 v[160:163], v[60:63], v[64:67], v[160:163]\n\t" \
    "s_add_u32 m0, s57, 0\n\t" \
    "v_mfma_f32_16x16x32_bf16 v[164:167], v[60:63], v[68:71], v[164:167]\n\t" \
    "ds_read_b64_tr_b16 v[56:57], v31 offset:160\n\t" \
    "v_mfma_f32_16x16x32_bf16 v[168:171], v[60:63], v[72:75], v[168:171]\n\t" \
    "buffer_load_dwordx4 v37, s[48:51], s63 offen lds\n\t" \
    "v_mfma_f32_16x16x32_bf16 v[172:175], v[60:63], v[76:79], v[172:175]\n\t" \
    "ds_read_b64_tr_b16 v[58:59], v31 offset:3232\n\t" \
    "s_waitcnt lgkmcnt(6)\n\t" \
    "v_mfma_f32_16x16x32_bf16 v[176:179], v[44:47], v[64:67], v[176:179]\n\t" \
    "ds_read_b64_tr_b16 v[60:61], v31 offset:6304\n\t" \
    "ds_read_b64_tr_b16 v[62:63], v31 offset:9376\n\t" \
    "v_mfma_f32_16x16x32_bf16 v[180:183], v[44:47], v[68:71], v[180:183]\n\t" \
    "s_add_u32 s60, s60, 131072\n\t" \
    "v_mfma_f32_16x16x32_bf16 v[184:187], v[44:47], v[72:75], v[184:187]\n\t" \
    "s_add_u32 s61, s61, s52\n\t" \
    "s_add_u32 s62, s62, s53\n\t" \
    "v_mfma_f32_16x16x32_bf16 v[188:191], v[44:47], v[76:79], v[188:191]\n\t" \
    "s_add_u32 s63, s63, s54\n\t" \
    "s_waitcnt lgkmcnt(6)\n\t" \
    "v_mfma_f32_16x16x32_bf16 v[192:195], v[48:51], v[64:67], v[192:195]\n\t" \
    "v_mfma_f32_16x16x32_bf16 v[196:199], v[48:51], v[68:71], v[196:199]\n\t" \
    "v_mfma_f32_16x16x32_bf16 v[200:203], v[48:51], v[72:75], v[200:203]\n\t" \
    "v_mfma_f32_16x16x32_bf16 v[204:207], v[48:51], v[76:79], v[204:207]\n\t" \
    "s_waitcnt vmcnt(10) lgkmcnt(0)\n\t" \
    "s_barrier\n\t" \
    "v_mfma_f32_16x16x32_bf16 v[208:211], v[52:55], v[64:67], v[208:211]\n\t" \
    "ds_read_b128 v[80:83], v34 offset:36864\n\t" \
    "ds_read_b64_tr_b16 v[44:45], v28 offset:36864\n\t" \
    "v_mfma_f32_16x16x32_bf16 v[212:215], v[52:55], v[68:71], v[212:215]\n\t" \
    "ds_read_b64_tr_b16 v[46:47], v28 offset:38912\n\t" \
    "v_mfma_f32_16x16x32_bf16 v[216:219], v[52:55], v[72:75], v[216:219]\n\t" \
    "ds_read_b128 v[84:87], v34 offset:37888\n\t" \
    "ds_read_b128 v[88:91], v34 offset:38912\n\t" \
    "v_mfma_f32_16x16x32_bf16 v[220:223], v[52:55], v[76:79], v[220:223]\n\t" \
    "ds_read_b128 v[92:95], v34 offset:39936\n\t" \
    "v_mfma_f32_16x16x32_bf16 v[224:227], v[56:59], v[64:67], v[224:227]\n\t" \
    "ds_read_b64_tr_b16 v[48:49], v29 offset:36864\n\t" \
    "v_mfma_f32_16x16x32_bf16 v[228:231], v[56:59], v[68:71], v[228:231]\n\t" \
    "ds_read_b64_tr_b16 v[50:51], v29 offset:38912\n\t" \
    "v_mfma_f32_16x16x32_bf16 v[232:235], v[56:59], v[72:75], v[232:235]\n\t" \
    "ds_read_b64_tr_b16 v[52:53], v28 offset:40960\n\t" \
    "v_mfma_f32_16x16x32_bf16 v[236:239], v[56:59], v[76:79], v[236:239]\n\t" \
    "ds_read_b64_tr_b16 v[54:55], v28 offset:43008\n\t" \
    "v_mfma_f32_16x16x32_bf16 v[240:243], v[60:63], v[64:67], v[240:243]\n\t" \
    "ds_read_b64_tr_b16 v[56:57], v29 offset:40960\n\t" \
    "v_mfma_f32_16x16x32_bf16 v[244:247], v[60:63], v[68:71], v[244:247]\n\t" \
    "v_mfma_f32_16x16x32_bf16 v[248:251], v[60:63], v[72:75], v[248:251]\n\t" \
    "ds_read_b64_tr_b16 v[58:59], v29 offset:43008\n\t" \
    "v_mfma_f32_16x16x32_bf16 v[252:255], v[60:63], v[76:79], v[252:255]\n\t" \
    "s_cmp_eq_u32 s64, 1\n\t" \
    "s_cbranch_scc0 LK2PF3_v1_%=\n\t" \
    "s_sub_u32 s55, s55, 73728\n\t" \
    "s_sub_u32 s56, s56, 73728\n\t" \
    "s_sub_u32 s58, s58, 73728\n\t" \
    "LK2PF3_v1_%=:\n\t" \
    "s_waitcnt lgkmcnt(6)\n\t" \
    "v_mfma_f32_16x16x32_bf16 v[96:99], v[44:47], v[80:83], v[96:99]\n\t" \
    "s_add_u32 m0, s58, 73728\n\t" \
    "v_mfma_f32_16x16x32_bf16 v[100:103], v[44:47], v[84:87], v[100:103]\n\t" \
    "ds_read_b64_tr_b16 v[60:61], v31 offset:36960\n\t" \
    "v_mfma_f32_16x16x32_bf16 v[104:107], v[44:47], v[88:91], v[104:107]\n\t" \
    "buffer_load_dwordx4 v38, s[36:39], s60 offen lds\n\t" \
    "v_mfma_f32_16x16x32_bf16 v[108:111], v[44:47], v[92:95], v[108:111]\n\t" \
    "ds_read_b64_tr_b16 v[62:63], v31 offset:40032\n\t" \
    "s_waitcnt lgkmcnt(6)\n\t" \
    "v_mfma_f32_16x16x32_bf16 v[112:115], v[48:51], v[80:83], v[112:115]\n\t" \
    "ds_read_b64_tr_b16 v[44:45], v31 offset:43104\n\t" \
    "v_mfma_f32_16x16x32_bf16 v[116:119], v[48:51], v[84:87], v[116:119]\n\t" \
    "buffer_load_dwordx4 v38, s[36:39], s60 offen offset:1024 lds\n\t" \
    "v_mfma_f32_16x16x32_bf16 v[120:123], v[48:51], v[88:91], v[120:123]\n\t" \
    "ds_read_b64_tr_b16 v[46:47], v31 offset:46176\n\t" \
    "v_mfma_f32_16x16x32_bf16 v[124:127], v[48:51], v[92:95], v[124:127]\n\t" \
    "s_waitcnt lgkmcnt(6)\n\t" \
    "v_mfma_f32_16x16x32_bf16 v[128:131], v[52:55], v[80:83], v[128:131]\n\t" \
    "s_add_u32 m0, s55, 73728\n\t" \
    "v_mfma_f32_16x16x32_bf16 v[132:135], v[52:55], v[84:87], v[132:135]\n\t" \
    "ds_read_b64_tr_b16 v[48:49], v30 offset:36992\n\t" \
    "v_mfma_f32_16x16x32_bf16 v[136:139], v[52:55], v[88:91], v[136:139]\n\t" \
    "buffer_load_dwordx4 v35, s[40:43], s61 offen lds\n\t" \
    "v_mfma_f32_16x16x32_bf16 v[140:143], v[52:55], v[92:95], v[140:143]\n\t" \
    "ds_read_b64_tr_b16 v[50:51], v30 offset:40064\n\t" \
    "s_waitcnt lgkmcnt(6)\n\t" \
    "v_mfma_f32_16x16x32_bf16 v[144:147], v[56:59], v[80:83], v[144:147]\n\t" \
    "s_add_u32 m0, s56, 73728\n\t" \
    "v_mfma_f32_16x16x32_bf16 v[148:151], v[56:59], v[84:87], v[148:151]\n\t" \
    "ds_read_b64_tr_b16 v[52:53], v30 offset:43136\n\t" \
    "v_mfma_f32_16x16x32_bf16 v[152:155], v[56:59], v[88:91], v[152:155]\n\t" \
    "buffer_load_dwordx4 v36, s[44:47], s62 offen lds\n\t" \
    "v_mfma_f32_16x16x32_bf16 v[156:159], v[56:59], v[92:95], v[156:159]\n\t" \
    "ds_read_b64_tr_b16 v[54:55], v30 offset:46208\n\t" \
    "s_waitcnt lgkmcnt(6)\n\t" \
    "v_mfma_f32_16x16x32_bf16 v[160:163], v[60:63], v[80:83], v[160:163]\n\t" \
    "s_add_u32 m0, s57, 0\n\t" \
    "v_mfma_f32_16x16x32_bf16 v[164:167], v[60:63], v[84:87], v[164:167]\n\t" \
    "ds_read_b64_tr_b16 v[56:57], v31 offset:37024\n\t" \
    "v_mfma_f32_16x16x32_bf16 v[168:171], v[60:63], v[88:91], v[168:171]\n\t" \
    "buffer_load_dwordx4 v37, s[48:51], s63 offen lds\n\t" \
    "v_mfma_f32_16x16x32_bf16 v[172:175], v[60:63], v[92:95], v[172:175]\n\t" \
    "ds_read_b64_tr_b16 v[58:59], v31 offset:40096\n\t" \
    "s_waitcnt lgkmcnt(6)\n\t" \
    "v_mfma_f32_16x16x32_bf16 v[176:179], v[44:47], v[80:83], v[176:179]\n\t" \
    "ds_read_b64_tr_b16 v[60:61], v31 offset:43168\n\t" \
    "ds_read_b64_tr_b16 v[62:63], v31 offset:46240\n\t" \
    "v_mfma_f32_16x16x32_bf16 v[180:183], v[44:47], v[84:87], v[180:183]\n\t" \
    "s_add_u32 s60, s60, 131072\n\t" \
    "v_mfma_f32_16x16x32_bf16 v[184:187], v[44:47], v[88:91], v[184:187]\n\t" \
    "s_add_u32 s61, s61, s52\n\t" \
    "s_add_u32 s62, s62, s53\n\t" \
    "v_mfma_f32_16x16x32_bf16 v[188:191], v[44:47], v[92:95], v[188:191]\n\t" \
    "s_add_u32 s63, s63, s54\n\t" \
    "s_waitcnt lgkmcnt(6)\n\t" \
    "v_mfma_f32_16x16x32_bf16 v[192:195], v[48:51], v[80:83], v[192:195]\n\t" \
    "v_subrev_u32 v28, 0x12000, v28\n\t" \
    "v_subrev_u32 v29, 0x12000, v29\n\t" \
    "v_mfma_f32_16x16x32_bf16 v[196:199], v[48:51], v[84:87], v[196:199]\n\t" \
    "v_subrev_u32 v30, 0x12000, v30\n\t" \
    "v_mfma_f32_16x16x32_bf16 v[200:203], v[48:51], v[88:91], v[200:203]\n\t" \
    "v_subrev_u32 v31, 0x12000, v31\n\t" \
    "v_mfma_f32_16x16x32_bf16 v[204:207], v[48:51], v[92:95], v[204:207]\n\t" \
    "v_subrev_u32 v34, 0x12000, v34\n\t" \
    "s_waitcnt vmcnt(10) lgkmcnt(0)\n\t" \
    "s_barrier\n\t" \
    "v_mfma_f32_16x16x32_bf16 v[208:211], v[52:55], v[80:83], v[208:211]\n\t" \
    "ds_read_b128 v[64:67], v34 offset:0\n\t" \
    "ds_read_b64_tr_b16 v[44:45], v28 offset:0\n\t" \
    "v_mfma_f32_16x16x32_bf16 v[212:215], v[52:55], v[84:87], v[212:215]\n\t" \
    "ds_read_b64_tr_b16 v[46:47], v28 offset:2048\n\t" \
    "v_mfma_f32_16x16x32_bf16 v[216:219], v[52:55], v[88:91], v[216:219]\n\t" \
    "ds_read_b128 v[68:71], v34 offset:1024\n\t" \
    "ds_read_b128 v[72:75], v34 offset:2048\n\t" \
    "v_mfma_f32_16x16x32_bf16 v[220:223], v[52:55], v[92:95], v[220:223]\n\t" \
    "ds_read_b128 v[76:79], v34 offset:3072\n\t" \
    "v_mfma_f32_16x16x32_bf16 v[224:227], v[56:59], v[80:83], v[224:227]\n\t" \
    "ds_read_b64_tr_b16 v[48:49], v29 offset:0\n\t" \
    "v_mfma_f32_16x16x32_bf16 v[228:231], v[56:59], v[84:87], v[228:231]\n\t" \
    "ds_read_b64_tr_b16 v[50:51], v29 offset:2048\n\t" \
    "v_mfma_f32_16x16x32_bf16 v[232:235], v[56:59], v[88:91], v[232:235]\n\t" \
    "ds_read_b64_tr_b16 v[52:53], v28 offset:4096\n\t" \
    "v_mfma_f32_16x16x32_bf16 v[236:239], v[56:59], v[92:95], v[236:239]\n\t" \
    "ds_read_b64_tr_b16 v[54:55], v28 offset:6144\n\t" \
    "v_mfma_f32_16x16x32_bf16 v[240:243], v[60:63], v[80:83], v[240:243]\n\t" \
    "ds_read_b64_tr_b16 v[56:57], v29 offset:4096\n\t" \
    "v_mfma_f32_16x16x32_bf16 v[244:247], v[60:63], v[84:87], v[244:247]\n\t" \
    "v_mfma_f32_16x16x32_bf16 v[248:251], v[60:63], v[88:91], v[248:251]\n\t" \
    "ds_read_b64_tr_b16 v[58:59], v29 offset:6144\n\t" \
    "v_mfma_f32_16x16x32_bf16 v[252:255], v[60:63], v[92:95], v[252:255]\n\t" \
    "s_sub_u32 s64, s64, 1\n\t" \
    "s_cmp_lg_u32 s64, 0\n\t" \
    "s_cbranch_scc1 LK2_v1_%=\n\t" \
    "LK2END_%=:\n\t" \
    "s_waitcnt lgkmcnt(0)\n\t" \
    "s_barrier\n\t" \
    "s_nop 7\n\t" \
    "s_nop 7\n\t"
#define K2_LOOP_Y \
    "s_cmp_eq_u32 s59, 1\n\t" \
    "s_cbranch_scc1 LK2V1_%=\n\t" \
    "s_nop 4\n\t" \
    "s_mov_b32 s60, 0\n\t" \
    "s_mov_b32 s61, 0\n\t" \
    "s_mov_b32 s62, 0\n\t" \
    "s_mov_b32 s63, 0\n\t" \
    "s_add_u32 m0, s58, 0\n\t" \
    "s_nop 0\n\t" \
    "buffer_load_dwordx4 v38, s[36:39], s60 offen lds\n\t" \
    "v_mov_b32 v96, 0\n\t" \
    "v_mov_b32 v97, 0\n\t" \
    "v_mov_b32 v98, 0\n\t" \
    "v_mov_b32 v99, 0\n\t" \
    "v_mov_b32 v100, 0\n\t" \
    "v_mov_b32 v101, 0\n\t" \
    "v_mov_b32 v102, 0\n\t" \
    "v_mov_b32 v103, 0\n\t" \
    "v_mov_b32 v104, 0\n\t" \
    "v_mov_b32 v105, 0\n\t" \
    "v_mov_b32 v106, 0\n\t" \
    "v_mov_b32 v107, 0\n\t" \
    "v_mov_b32 v108, 0\n\t" \
    "v_mov_b32 v109, 0\n\t" \
    "v_mov_b32 v110, 0\n\t" \
    "v_mov_b32 v111, 0\n\t" \
    "buffer_load_dwordx4 v38, s[36:39], s60 offen offset:1024 lds\n\t" \
    "v_mov_b32 v112, 0\n\t" \
    "v_mov_b32 v113, 0\n\t" \
    "v_mov_b32 v114, 0\n\t" \
    "v_mov_b32 v115, 0\n\t" \
    "v_mov_b32 v116, 0\n\t" \
    "v_mov_b32 v117, 0\n\t" \
    "v_mov_b32 v118, 0\n\t" \
    "v_mov_b32 v119, 0\n\t" \
    "v_mov_b32 v120, 0\n\t" \
    "v_mov_b32 v121, 0\n\t" \
    "v_mov_b32 v122, 0\n\t" \
    "v_mov_b32 v123, 0\n\t" \
    "v_mov_b32 v124, 0\n\t" \
    "v_mov_b32 v125, 0\n\t" \
    "v_mov_b32 v126, 0\n\t" \
    "v_mov_b32 v127, 0\n\t" \
    "s_add_u32 m0, s55, 0\n\t" \
    "s_nop 0\n\t" \
    "buffer_load_dwordx4 v35, s[40:43], s61 offen lds\n\t" \
    "v_mov_b32 v128, 0\n\t" \
    "v_mov_b32 v129, 0\n\t" \
    "v_mov_b32 v130, 0\n\t" \
    "v_mov_b32 v131, 0\n\t" \
    "v_mov_b32 v132, 0\n\t" \
    "v_mov_b32 v133, 0\n\t" \
    "v_mov_b32 v134, 0\n\t" \
    "v_mov_b32 v135, 0\n\t" \
    "v_mov_b32 v136, 0\n\t" \
    "v_mov_b32 v137, 0\n\t" \
    "v_mov_b32 v138, 0\n\t" \
    "v_mov_b32 v139, 0\n\t" \
    "v_mov_b32 v140, 0\n\t" \
    "v_mov_b32 v141, 0\n\t" \
    "v_mov_b32 v142, 0\n\t" \
    "v_mov_b32 v143, 0\n\t" \
    "s_add_u32 m0, s56, 0\n\t" \
    "s_nop 0\n\t" \
    "buffer_load_dwordx4 v36, s[44:47], s62 offen lds\n\t" \
    "v_mov_b32 v144, 0\n\t" \
    "v_mov_b32 v145, 0\n\t" \
    "v_mov_b32 v146, 0\n\t" \
    "v_mov_b32 v147, 0\n\t" \
    "v_mov_b32 v148, 0\n\t" \
    "v_mov_b32 v149, 0\n\t" \
    "v_mov_b32 v150, 0\n\t" \
    "v_mov_b32 v151, 0\n\t" \
    "v_mov_b32 v152, 0\n\t" \
    "v_mov_b32 v153, 0\n\t" \
    "v_mov_b32 v154, 0\n\t" \
    "v_mov_b32 v155, 0\n\t" \
    "v_mov_b32 v156, 0\n\t" \
    "v_mov_b32 v157, 0\n\t" \
    "v_mov_b32 v158, 0\n\t" \
    "v_mov_b32 v159, 0\n\t" \
    "s_add_u32 m0, s57, 0\n\t" \
    "s_nop 0\n\t" \
    "buffer_load_dwordx4 v37, s[48:51], s63 offen lds\n\t" \
    "v_mov_b32 v160, 0\n\t" \
    "v_mov_b32 v161, 0\n\t" \
    "v_mov_b32 v162, 0\n\t" \
    "v_mov_b32 v163, 0\n\t" \
    "v_mov_b32 v164, 0\n\t" \
    "v_mov_b32 v165, 0\n\t" \
    "v_mov_b32 v166, 0\n\t" \
    "v_mov_b32 v167, 0\n\t" \
    "v_mov_b32 v168, 0\n\t" \
    "v_mov_b32 v169, 0\n\t" \
    "v_mov_b32 v170, 0\n\t" \
    "v_mov_b32 v171, 0\n\t" \
    "v_mov_b32 v172, 0\n\t" \
    "v_mov_b32 v173, 0\n\t" \
    "v_mov_b32 v174, 0\n\t" \
    "v_mov_b32 v175, 0\n\t" \
    "s_add_u32 s60, s60, 131072\n\t" \
    "s_add_u32 s61, s61, s52\n\t" \
    "s_add_u32 s62, s62, s53\n\t" \
    "s_add_u32 s63, s63, s54\n\t" \
    "s_add_u32 m0, s58, 36864\n\t" \
    "s_nop 0\n\t" \
    "buffer_load_dwordx4 v38, s[36:39], s60 offen lds\n\t" \
    "v_mov_b32 v176, 0\n\t" \
    "v_mov_b32 v177, 0\n\t" \
    "v_mov_b32 v178, 0\n\t" \
    "v_mov_b32 v179, 0\n\t" \
    "v_mov_b32 v180, 0\n\t" \
    "v_mov_b32 v181, 0\n\t" \
    "v_mov_b32 v182, 0\n\t" \
    "v_mov_b32 v183, 0\n\t" \
    "v_mov_b32 v184, 0\n\t" \
    "v_mov_b32 v185, 0\n\t" \
    "v_mov_b32 v186, 0\n\t" \
    "v_mov_b32 v187, 0\n\t" \
    "v_mov_b32 v188, 0\n\t" \
    "v_mov_b32 v189, 0\n\t" \
    "v_mov_b32 v190, 0\n\t" \
    "v_mov_b32 v191, 0\n\t" \
    "buffer_load_dwordx4 v38, s[36:39], s60 offen offset:1024 lds\n\t" \
    "v_mov_b32 v192, 0\n\t" \
    "v_mov_b32 v193, 0\n\t" \
    "v_mov_b32 v194, 0\n\t" \
    "v_mov_b32 v195, 0\n\t" \
    "v_mov_b32 v196, 0\n\t" \
    "v_mov_b32 v197, 0\n\t" \
    "v_mov_b32 v198, 0\n\t" \
    "v_mov_b32 v199, 0\n\t" \
    "v_mov_b32 v200, 0\n\t" \
    "v_mov_b32 v201, 0\n\t" \
    "v_mov_b32 v202, 0\n\t" \
    "v_mov_b32 v203, 0\n\t" \
    "v_mov_b32 v204, 0\n\t" \
    "v_mov_b32 v205, 0\n\t" \
    "v_mov_b32 v206, 0\n\t" \
    "v_mov_b32 v207, 0\n\t" \
    "s_add_u32 m0, s55, 36864\n\t" \
    "s_nop 0\n\t" \
    "buffer_load_dwordx4 v35, s[40:43], s61 offen lds\n\t" \
    "v_mov_b32 v208, 0\n\t" \
    "v_mov_b32 v209, 0\n\t" \
    "v_mov_b32 v210, 0\n\t" \
    "v_mov_b32 v211, 0\n\t" \
    "v_mov_b32 v212, 0\n\t" \
    "v_mov_b32 v213, 0\n\t" \
    "v_mov_b32 v214, 0\n\t" \
    "v_mov_b32 v215, 0\n\t" \
    "v_mov_b32 v216, 0\n\t" \
    "v_mov_b32 v217, 0\n\t" \
    "v_mov_b32 v218, 0\n\t" \
    "v_mov_b32 v219, 0\n\t" \
    "v_mov_b32 v220, 0\n\t" \
    "v_mov_b32 v221, 0\n\t" \
    "v_mov_b32 v222, 0\n\t" \
    "v_mov_b32 v223, 0\n\t" \
    "s_add_u32 m0, s56, 36864\n\t" \
    "s_nop 0\n\t" \
    "buffer_load_dwordx4 v36, s[44:47], s62 offen lds\n\t" \
    "v_mov_b32 v224, 0\n\t" \
    "v_mov_b32 v225, 0\n\t" \
    "v_mov_b32 v226, 0\n\t" \
    "v_mov_b32 v227, 0\n\t" \
    "v_mov_b32 v228, 0\n\t" \
    "v_mov_b32 v229, 0\n\t" \
    "v_mov_b32 v230, 0\n\t" \
    "v_mov_b32 v231, 0\n\t" \
    "v_mov_b32 v232, 0\n\t" \
    "v_mov_b32 v233, 0\n\t" \
    "v_mov_b32 v234, 0\n\t" \
    "v_mov_b32 v235, 0\n\t" \
    "v_mov_b32 v236, 0\n\t" \
    "v_mov_b32 v237, 0\n\t" \
    "v_mov_b32 v238, 0\n\t" \
    "v_mov_b32 v239, 0\n\t" \
    "s_add_u32 m0, s57, 36864\n\t" \
    "s_nop 0\n\t" \
    "buffer_load_dwordx4 v37, s[48:51], s63 offen lds\n\t" \
    "v_mov_b32 v240, 0\n\t" \
    "v_mov_b32 v241, 0\n\t" \
    "v_mov_b32 v242, 0\n\t" \
    "v_mov_b32 v243, 0\n\t" \
    "v_mov_b32 v244, 0\n\t" \
    "v_mov_b32 v245, 0\n\t" \
    "v_mov_b32 v246, 0\n\t" \
    "v_mov_b32 v247, 0\n\t" \
    "v_mov_b32 v248, 0\n\t" \
    "v_mov_b32 v249, 0\n\t" \
    "v_mov_b32 v250, 0\n\t" \
    "v_mov_b32 v251, 0\n\t" \
    "v_mov_b32 v252, 0\n\t" \
    "v_mov_b32 v253, 0\n\t" \
    "v_mov_b32 v254, 0\n\t" \
    "v_mov_b32 v255, 0\n\t" \
    "s_add_u32 s60, s60, 131072\n\t" \
    "s_add_u32 s61, s61, s52\n\t" \
    "s_add_u32 s62, s62, s53\n\t" \
    "s_add_u32 s63, s63, s54\n\t" \
    "s_mov_b32 s64, 8\n\t" \
    "s_waitcnt vmcnt(5)\n\t" \
    "s_barrier\n\t" \
    "ds_read_b128 v[64:67], v34 offset:0\n\t" \
    "ds_read_b128 v[68:71], v34 offset:1024\n\t" \
    "ds_read_b128 v[72:75], v34 offset:2048\n\t" \
    "ds_read_b128 v[76:79], v34 offset:3072\n\t" \
    "ds_read_b64_tr_b16 v[44:45], v28 offset:0\n\t" \
    "ds_read_b64_tr_b16 v[46:47], v28 offset:2048\n\t" \
    "ds_read_b64_tr_b16 v[48:49], v29 offset:0\n\t" \
    "ds_read_b64_tr_b16 v[50:51], v29 offset:2048\n\t" \
    "ds_read_b64_tr_b16 v[52:53], v30 offset:0\n\t" \
    "ds_read_b64_tr_b16 v[54:55], v30 offset:2048\n\t" \
    "ds_read_b64_tr_b16 v[56:57], v30 offset:8192\n\t" \
    "ds_read_b64_tr_b16 v[58:59], v30 offset:10240\n\t" \
    "s_add_u32 m0, s58, 73728\n\t" \
    "s_nop 0\n\t" \
    "buffer_load_dwordx4 v38, s[36:39], s60 offen lds\n\t" \
    "buffer_load_dwordx4 v38, s[36:39], s60 offen offset:1024 lds\n\t" \
    "s_add_u32 m0, s55, 73728\n\t" \
    "s_nop 0\n\t" \
    "buffer_load_dwordx4 v35, s[40:43], s61 offen lds\n\t" \
    "s_add_u32 m0, s56, 73728\n\t" \
    "s_nop 0\n\t" \
    "buffer_load_dwordx4 v36, s[44:47], s62 offen lds\n\t" \
    "s_add_u32 m0, s57, 73728\n\t" \
    "s_nop 0\n\t" \
    "buffer_load_dwordx4 v37, s[48:51], s63 offen lds\n\t" \
    "s_add_u32 s60, s60, 131072\n\t" \
    "s_add_u32 s61, s61, s52\n\t" \
    "s_add_u32 s62, s62, s53\n\t" \
    "s_add_u32 s63, s63, s54\n\t" \
    "s_cmp_eq_u32 s73, 0\n\t" \
    "s_cbranch_scc1 LK2NP_v0_%=\n\t" \
    "s_add_u32 m0, s72, 155648\n\t" \
    "s_nop 0\n\t" \
    "global_load_lds_dword v[24:25], off\n\t" \
    "LK2NP_v0_%=:\n\t" \
    "LK2_v0_%=:\n\t" \
    "s_waitcnt lgkmcnt(6)\n\t" \
    "v_mfma_f32_16x16x32_bf16 v[96:99], v[44:47], v[64:67], v[96:99]\n\t" \
    "s_add_u32 m0, s58, 110592\n\t" \
    "v_mfma_f32_16x16x32_bf16 v[100:103], v[44:47], v[68:71], v[100:103]\n\t" \
    "ds_read_b64_tr_b16 v[60:61], v31 offset:0\n\t" \
    "v_mfma_f32_16x16x32_bf16 v[104:107], v[44:47], v[72:75], v[104:107]\n\t" \
    "buffer_load_dwordx4 v38, s[36:39], s60 offen lds\n\t" \
    "v_mfma_f32_16x16x32_bf16 v[108:111], v[44:47], v[76:79], v[108:111]\n\t" \
    "ds_read_b64_tr_b16 v[62:63], v31 offset:2048\n\t" \
    "s_waitcnt lgkmcnt(6)\n\t" \
    "v_mfma_f32_16x16x32_bf16 v[112:115], v[48:51], v[64:67], v[112:115]\n\t" \
    "ds_read_b64_tr_b16 v[44:45], v31 offset:8192\n\t" \
    "v_mfma_f32_16x16x32_bf16 v[116:119], v[48:51], v[68:71], v[116:119]\n\t" \
    "buffer_load_dwordx4 v38, s[36:39], s60 offen offset:1024 lds\n\t" \
    "v_mfma_f32_16x16x32_bf16 v[120:123], v[48:51], v[72:75], v[120:123]\n\t" \
    "ds_read_b64_tr_b16 v[46:47], v31 offset:10240\n\t" \
    "v_mfma_f32_16x16x32_bf16 v[124:127], v[48:51], v[76:79], v[124:127]\n\t" \
    "s_waitcnt lgkmcnt(6)\n\t" \
    "v_mfma_f32_16x16x32_bf16 v[128:131], v[52:55], v[64:67], v[128:131]\n\t" \
    "s_add_u32 m0, s55, 110592\n\t" \
    "v_mfma_f32_16x16x32_bf16 v[132:135], v[52:55], v[68:71], v[132:135]\n\t" \
    "ds_read_b64_tr_b16 v[48:49], v32 offset:0\n\t" \
    "v_mfma_f32_16x16x32_bf16 v[136:139], v[52:55], v[72:75], v[136:139]\n\t" \
    "buffer_load_dwordx4 v35, s[40:43], s61 offen lds\n\t" \
    "v_mfma_f32_16x16x32_bf16 v[140:143], v[52:55], v[76:79], v[140:143]\n\t" \
    "ds_read_b64_tr_b16 v[50:51], v32 offset:2048\n\t" \
    "s_waitcnt lgkmcnt(6)\n\t" \
    "v_mfma_f32_16x16x32_bf16 v[144:147], v[56:59], v[64:67], v[144:147]\n\t" \
    "s_add_u32 m0, s56, 110592\n\t" \
    "v_mfma_f32_16x16x32_bf16 v[148:151], v[56:59], v[68:71], v[148:151]\n\t" \
    "ds_read_b64_tr_b16 v[52:53], v32 offset:8192\n\t" \
    "v_mfma_f32_16x16x32_bf16 v[152:155], v[56:59], v[72:75], v[152:155]\n\t" \
    "buffer_load_dwordx4 v36, s[44:47], s62 offen lds\n\t" \
    "v_mfma_f32_16x16x32_bf16 v[156:159], v[56:59], v[76:79], v[156:159]\n\t" \
    "ds_read_b64_tr_b16 v[54:55], v32 offset:10240\n\t" \
    "s_waitcnt lgkmcnt(6)\n\t" \
    "v_mfma_f32_16x16x32_bf16 v[160:163], v[60:63], v[64:67], v[160:163]\n\t" \
    "s_add_u32 m0, s57, 110592\n\t" \
    "v_mfma_f32_16x16x32_bf16 v[164:167], v[60:63], v[68:71], v[164:167]\n\t" \
    "ds_read_b64_tr_b16 v[56:57], v33 offset:0\n\t" \
    "v_mfma_f32_16x16x32_bf16 v[168:171], v[60:63], v[72:75], v[168:171]\n\t" \
    "buffer_load_dwordx4 v37, s[48:51], s63 offen lds\n\t" \
    "v_mfma_f32_16x16x32_bf16 v[172:175], v[60:63], v[76:79], v[172:175]\n\t" \
    "ds_read_b64_tr_b16 v[58:59], v33 offset:2048\n\t" \
    "s_waitcnt lgkmcnt(6)\n\t" \
    "v_mfma_f32_16x16x32_bf16 v[176:179], v[44:47], v[64:67], v[176:179]\n\t" \
    "ds_read_b64_tr_b16 v[60:61], v33 offset:8192\n\t" \
    "ds_read_b64_tr_b16 v[62:63], v33 offset:10240\n\t" \
    "v_mfma_f32_16x16x32_bf16 v[180:183], v[44:47], v[68:71], v[180:183]\n\t" \
    "s_add_u32 s60, s60, 131072\n\t" \
    "v_mfma_f32_16x16x32_bf16 v[184:187], v[44:47], v[72:75], v[184:187]\n\t" \
    "s_add_u32 s61, s61, s52\n\t" \
    "s_add_u32 s62, s62, s53\n\t" \
    "v_mfma_f32_16x16x32_bf16 v[188:191], v[44:47], v[76:79], v[188:191]\n\t" \
    "s_add_u32 s63, s63, s54\n\t" \
    "s_waitcnt lgkmcnt(6)\n\t" \
    "v_mfma_f32_16x16x32_bf16 v[192:195], v[48:51], v[64:67], v[192:195]\n\t" \
    "v_mfma_f32_16x16x32_bf16 v[196:199], v[48:51], v[68:71], v[196:199]\n\t" \
    "v_mfma_f32_16x16x32_bf16 v[200:203], v[48:51], v[72:75], v[200:203]\n\t" \
    "v_mfma_f32_16x16x32_bf16 v[204:207], v[48:51], v[76:79], v[204:207]\n\t" \
    "s_waitcnt vmcnt(10) lgkmcnt(0)\n\t" \
    "s_barrier\n\t" \
    "v_mfma_f32_16x16x32_bf16 v[208:211], v[52:55], v[64:67], v[208:211]\n\t" \
    "ds_read_b128 v[80:83], v34 offset:36864\n\t" \
    "ds_read_b64_tr_b16 v[44:45], v28 offset:36864\n\t" \
    "v_mfma_f32_16x16x32_bf16 v[212:215], v[52:55], v[68:71], v[212:215]\n\t" \
    "ds_read_b64_tr_b16 v[46:47], v28 offset:38912\n\t" \
    "v_mfma_f32_16x16x32_bf16 v[216:219], v[52:55], v[72:75], v[216:219]\n\t" \
    "ds_read_b128 v[84:87], v34 offset:37888\n\t" \
    "ds_read_b128 v[88:91], v34 offset:38912\n\t" \
    "v_mfma_f32_16x16x32_bf16 v[220:223], v[52:55], v[76:79], v[220:223]\n\t" \
    "ds_read_b128 v[92:95], v34 offset:39936\n\t" \
    "v_mfma_f32_16x16x32_bf16 v[224:227], v[56:59], v[64:67], v[224:227]\n\t" \
    "ds_read_b64_tr_b16 v[48:49], v29 offset:36864\n\t" \
    "v_mfma_f32_16x16x32_bf16 v[228:231], v[56:59], v[68:71], v[228:231]\n\t" \
    "ds_read_b64_tr_b16 v[50:51], v29 offset:38912\n\t" \
    "v_mfma_f32_16x16x32_bf16 v[232:235], v[56:59], v[72:75], v[232:235]\n\t" \
    "ds_read_b64_tr_b16 v[52:53], v30 offset:36864\n\t" \
    "v_mfma_f32_16x16x32_bf16 v[236:239], v[56:59], v[76:79], v[236:239]\n\t" \
    "ds_read_b64_tr_b16 v[54:55], v30 offset:38912\n\t" \
    "v_mfma_f32_16x16x32_bf16 v[240:243], v[60:63], v[64:67], v[240:243]\n\t" \
    "ds_read_b64_tr_b16 v[56:57], v30 offset:45056\n\t" \
    "v_mfma_f32_16x16x32_bf16 v[244:247], v[60:63], v[68:71], v[244:247]\n\t" \
    "v_mfma_f32_16x16x32_bf16 v[248:251], v[60:63], v[72:75], v[248:251]\n\t" \
    "ds_read_b64_tr_b16 v[58:59], v30 offset:47104\n\t" \
    "v_mfma_f32_16x16x32_bf16 v[252:255], v[60:63], v[76:79], v[252:255]\n\t" \
    "s_cmp_eq_u32 s64, 1\n\t" \
    "s_cbranch_scc0 LK2PF1_v0_%=\n\t" \
    "s_mov_b32 s40, s68\n\t" \
    "s_mov_b32 s41, s69\n\t" \
    "s_mov_b32 s42, 7340032\n\t" \
    "s_mov_b32 s52, 4194304\n\t" \
    "s_mov_b32 s61, 0\n\t" \
    "v_mov_b32 v35, v39\n\t" \
    "s_mov_b32 s55, s72\n\t" \
    "s_mov_b32 s44, s68\n\t" \
    "s_mov_b32 s45, s69\n\t" \
    "s_mov_b32 s46, 7340032\n\t" \
    "s_mov_b32 s53, 4194304\n\t" \
    "s_mov_b32 s62, 2097152\n\t" \
    "v_mov_b32 v36, v39\n\t" \
    "s_mov_b32 s56, s72\n\t" \
    "s_mov_b32 s48, s70\n\t" \
    "s_mov_b32 s49, s71\n\t" \
    "s_mov_b32 s50, 2097152\n\t" \
    "s_mov_b32 s54, 1048576\n\t" \
    "s_mov_b32 s63, 0\n\t" \
    "v_mov_b32 v37, v40\n\t" \
    "s_mov_b32 s57, s72\n\t" \
    "s_mov_b32 s58, s72\n\t" \
    "LK2PF1_v0_%=:\n\t" \
    "s_waitcnt lgkmcnt(6)\n\t" \
    "v_mfma_f32_16x16x32_bf16 v[96:99], v[44:47], v[80:83], v[96:99]\n\t" \
    "s_add_u32 m0, s58, 0\n\t" \
    "v_mfma_f32_16x16x32_bf16 v[100:103], v[44:47], v[84:87], v[100:103]\n\t" \
    "ds_read_b64_tr_b16 v[60:61], v31 offset:36864\n\t" \
    "v_mfma_f32_16x16x32_bf16 v[104:107], v[44:47], v[88:91], v[104:107]\n\t" \
    "buffer_load_dwordx4 v38, s[36:39], s60 offen lds\n\t" \
    "v_mfma_f32_16x16x32_bf16 v[108:111], v[44:47], v[92:95], v[108:111]\n\t" \
    "ds_read_b64_tr_b16 v[62:63], v31 offset:38912\n\t" \
    "s_waitcnt lgkmcnt(6)\n\t" \
    "v_mfma_f32_16x16x32_bf16 v[112:115], v[48:51], v[80:83], v[112:115]\n\t" \
    "ds_read_b64_tr_b16 v[44:45], v31 offset:45056\n\t" \
    "v_mfma_f32_16x16x32_bf16 v[116:119], v[48:51], v[84:87], v[116:119]\n\t" \
    "buffer_load_dwordx4 v38, s[36:39], s60 offen offset:1024 lds\n\t" \
    "v_mfma_f32_16x16x32_bf16 v[120:123], v[48:51], v[88:91], v[120:123]\n\t" \
    "ds_read_b64_tr_b16 v[46:47], v31 offset:47104\n\t" \
    "v_mfma_f32_16x16x32_bf16 v[124:127], v[48:51], v[92:95], v[124:127]\n\t" \
    "s_waitcnt lgkmcnt(6)\n\t" \
    "v_mfma_f32_16x16x32_bf16 v[128:131], v[52:55], v[80:83], v[128:131]\n\t" \
    "s_add_u32 m0, s55, 0\n\t" \
    "v_mfma_f32_16x16x32_bf16 v[132:135], v[52:55], v[84:87], v[132:135]\n\t" \
    "ds_read_b64_tr_b16 v[48:49], v32 offset:36864\n\t" \
    "v_mfma_f32_16x16x32_bf16 v[136:139], v[52:55], v[88:91], v[136:139]\n\t" \
    "buffer_load_dwordx4 v35, s[40:43], s61 offen lds\n\t" \
    "v_mfma_f32_16x16x32_bf16 v[140:143], v[52:55], v[92:95], v[140:143]\n\t" \
    "ds_read_b64_tr_b16 v[50:51], v32 offset:38912\n\t" \
    "s_waitcnt lgkmcnt(6)\n\t" \
    "v_mfma_f32_16x16x32_bf16 v[144:147], v[56:59], v[80:83], v[144:147]\n\t" \
    "s_add_u32 m0, s56, 0\n\t" \
    "v_mfma_f32_16x16x32_bf16 v[148:151], v[56:59], v[84:87], v[148:151]\n\t" \
    "ds_read_b64_tr_b16 v[52:53], v32 offset:45056\n\t" \
    "v_mfma_f32_16x16x32_bf16 v[152:155], v[56:59], v[88:91], v[152:155]\n\t" \
    "buffer_load_dwordx4 v36, s[44:47], s62 offen lds\n\t" \
    "v_mfma_f32_16x16x32_bf16 v[156:159], v[56:59], v[92:95], v[156:159]\n\t" \
    "ds_read_b64_tr_b16 v[54:55], v32 offset:47104\n\t" \
    "s_waitcnt lgkmcnt(6)\n\t" \
    "v_mfma_f32_16x16x32_bf16 v[160:163], v[60:63], v[80:83], v[160:163]\n\t" \
    "s_add_u32 m0, s57, 0\n\t" \
    "v_mfma_f32_16x16x32_bf16 v[164:167], v[60:63], v[84:87], v[164:167]\n\t" \
    "ds_read_b64_tr_b16 v[56:57], v33 offset:36864\n\t" \
    "v_mfma_f32_16x16x32_bf16 v[168:171], v[60:63], v[88:91], v[168:171]\n\t" \
    "buffer_load_dwordx4 v37, s[48:51], s63 offen lds\n\t" \
    "v_mfma_f32_16x16x32_bf16 v[172:175], v[60:63], v[92:95], v[172:175]\n\t" \
    "ds_read_b64_tr_b16 v[58:59], v33 offset:38912\n\t" \
    "s_waitcnt lgkmcnt(6)\n\t" \
    "v_mfma_f32_16x16x32_bf16 v[176:179], v[44:47], v[80:83], v[176:179]\n\t" \
    "ds_read_b64_tr_b16 v[60:61], v33 offset:45056\n\t" \
    "ds_read_b64_tr_b16 v[62:63], v33 offset:47104\n\t" \
    "v_mfma_f32_16x16x32_bf16 v[180:183], v[44:47], v[84:87], v[180:183]\n\t" \
    "s_add_u32 s60, s60, 131072\n\t" \
    "v_mfma_f32_16x16x32_bf16 v[184:187], v[44:47], v[88:91], v[184:187]\n\t" \
    "s_add_u32 s61, s61, s52\n\t" \
    "s_add_u32 s62, s62, s53\n\t" \
    "v_mfma_f32_16x16x32_bf16 v[188:191], v[44:47], v[92:95], v[188:191]\n\t" \
    "s_add_u32 s63, s63, s54\n\t" \
    "s_waitcnt lgkmcnt(6)\n\t" \
    "v_mfma_f32_16x16x32_bf16 v[192:195], v[48:51], v[80:83], v[192:195]\n\t" \
    "v_add_u32 v28, 0x12000, v28\n\t" \
    "v_add_u32 v29, 0x12000, v29\n\t" \
    "v_mfma_f32_16x16x32_bf16 v[196:199], v[48:51], v[84:87], v[196:199]\n\t" \
    "v_add_u32 v30, 0x12000, v30\n\t" \
    "v_add_u32 v31, 0x12000, v31\n\t" \
    "v_mfma_f32_16x16x32_bf16 v[200:203], v[48:51], v[88:91], v[200:203]\n\t" \
    "v_add_u32 v32, 0x12000, v32\n\t" \
    "v_add_u32 v33, 0x12000, v33\n\t" \
    "v_mfma_f32_16x16x32_bf16 v[204:207], v[48:51], v[92:95], v[204:207]\n\t" \
    "v_add_u32 v34, 0x12000, v34\n\t" \
    "s_waitcnt vmcnt(10) lgkmcnt(0)\n\t" \
    "s_barrier\n\t" \
    "v_mfma_f32_16x16x32_bf16 v[208:211], v[52:55], v[80:83], v[208:211]\n\t" \
    "ds_read_b128 v[64:67], v34 offset:0\n\t" \
    "ds_read_b64_tr_b16 v[44:45], v28 offset:0\n\t" \
    "v_mfma_f32_16x16x32_bf16 v[212:215], v[52:55], v[84:87], v[212:215]\n\t" \
    "ds_read_b64_tr_b16 v[46:47], v28 offset:2048\n\t" \
    "v_mfma_f32_16x16x32_bf16 v[216:219], v[52:55], v[88:91], v[216:219]\n\t" \
    "ds_read_b128 v[68:71], v34 offset:1024\n\t" \
    "ds_read_b128 v[72:75], v34 offset:2048\n\t" \
    "v_mfma_f32_16x16x32_bf16 v[220:223], v[52:55], v[92:95], v[220:223]\n\t" \
    "ds_read_b128 v[76:79], v34 offset:3072\n\t" \
    "v_mfma_f32_16x16x32_bf16 v[224:227], v[56:59], v[80:83], v[224:227]\n\t" \
    "ds_read_b64_tr_b16 v[48:49], v29 offset:0\n\t" \
    "v_mfma_f32_16x16x32_bf16 v[228:231], v[56:59], v[84:87], v[228:231]\n\t" \
    "ds_read_b64_tr_b16 v[50:51], v29 offset:2048\n\t" \
    "v_mfma_f32_16x16x32_bf16 v[232:235], v[56:59], v[88:91], v[232:235]\n\t" \
    "ds_read_b64_tr_b16 v[52:53], v30 offset:0\n\t" \
    "v_mfma_f32_16x16x32_bf16 v[236:239], v[56:59], v[92:95], v[236:239]\n\t" \
    "ds_read_b64_tr_b16 v[54:55], v30 offset:2048\n\t" \
    "v_mfma_f32_16x16x32_bf16 v[240:243], v[60:63], v[80:83], v[240:243]\n\t" \
    "ds_read_b64_tr_b16 v[56:57], v30 offset:8192\n\t" \
    "v_mfma_f32_16x16x32_bf16 v[244:247], v[60:63], v[84:87], v[244:247]\n\t" \
    "v_mfma_f32_16x16x32_bf16 v[248:251], v[60:63], v[88:91], v[248:251]\n\t" \
    "ds_read_b64_tr_b16 v[58:59], v30 offset:10240\n\t" \
    "v_mfma_f32_16x16x32_bf16 v[252:255], v[60:63], v[92:95], v[252:255]\n\t" \
    "s_waitcnt lgkmcnt(6)\n\t" \
    "v_mfma_f32_16x16x32_bf16 v[96:99], v[44:47], v[64:67], v[96:99]\n\t" \
    "s_add_u32 m0, s58, 36864\n\t" \
    "v_mfma_f32_16x16x32_bf16 v[100:103], v[44:47], v[68:71], v[100:103]\n\t" \
    "ds_read_b64_tr_b16 v[60:61], v31 offset:0\n\t" \
    "v_mfma_f32_16x16x32_bf16 v[104:107], v[44:47], v[72:75], v[104:107]\n\t" \
    "buffer_load_dwordx4 v38, s[36:39], s60 offen lds\n\t" \
    "v_mfma_f32_16x16x32_bf16 v[108:111], v[44:47], v[76:79], v[108:111]\n\t" \
    "ds_read_b64_tr_b16 v[62:63], v31 offset:2048\n\t" \
    "s_waitcnt lgkmcnt(6)\n\t" \
    "v_mfma_f32_16x16x32_bf16 v[112:115], v[48:51], v[64:67], v[112:115]\n\t" \
    "ds_read_b64_tr_b16 v[44:45], v31 offset:8192\n\t" \
    "v_mfma_f32_16x16x32_bf16 v[116:119], v[48:51], v[68:71], v[116:119]\n\t" \
    "buffer_load_dwordx4 v38, s[36:39], s60 offen offset:1024 lds\n\t" \
    "v_mfma_f32_16x16x32_bf16 v[120:123], v[48:51], v[72:75], v[120:123]\n\t" \
    "ds_read_b64_tr_b16 v[46:47], v31 offset:10240\n\t" \
    "v_mfma_f32_16x16x32_bf16 v[124:127], v[48:51], v[76:79], v[124:127]\n\t" \
    "s_waitcnt lgkmcnt(6)\n\t" \
    "v_mfma_f32_16x16x32_bf16 v[128:131], v[52:55], v[64:67], v[128:131]\n\t" \
    "s_add_u32 m0, s55, 36864\n\t" \
    "v_mfma_f32_16x16x32_bf16 v[132:135], v[52:55], v[68:71], v[132:135]\n\t" \
    "ds_read_b64_tr_b16 v[48:49], v32 offset:0\n\t" \
    "v_mfma_f32_16x16x32_bf16 v[136:139], v[52:55], v[72:75], v[136:139]\n\t" \
    "buffer_load_dwordx4 v35, s[40:43], s61 offen lds\n\t" \
    "v_mfma_f32_16x16x32_bf16 v[140:143], v[52:55], v[76:79], v[140:143]\n\t" \
    "ds_read_b64_tr_b16 v[50:51], v32 offset:2048\n\t" \
    "s_waitcnt lgkmcnt(6)\n\t" \
    "v_mfma_f32_16x16x32_bf16 v[144:147], v[56:59], v[64:67], v[144:147]\n\t" \
    "s_add_u32 m0, s56, 36864\n\t" \
    "v_mfma_f32_16x16x32_bf16 v[148:151], v[56:59], v[68:71], v[148:151]\n\t" \
    "ds_read_b64_tr_b16 v[52:53], v32 offset:8192\n\t" \
    "v_mfma_f32_16x16x32_bf16 v[152:155], v[56:59], v[72:75], v[152:155]\n\t" \
    "buffer_load_dwordx4 v36, s[44:47], s62 offen lds\n\t" \
    "v_mfma_f32_16x16x32_bf16 v[156:159], v[56:59], v[76:79], v[156:159]\n\t" \
    "ds_read_b64_tr_b16 v[54:55], v32 offset:10240\n\t" \
    "s_waitcnt lgkmcnt(6)\n\t" \
    "v_mfma_f32_16x16x32_bf16 v[160:163], v[60:63], v[64:67], v[160:163]\n\t" \
    "s_add_u32 m0, s57, 36864\n\t" \
    "v_mfma_f32_16x16x32_bf16 v[164:167], v[60:63], v[68:71], v[164:167]\n\t" \
    "ds_read_b64_tr_b16 v[56:57], v33 offset:0\n\t" \
    "v_mfma_f32_16x16x32_bf16 v[168:171], v[60:63], v[72:75], v[168:171]\n\t" \
    "buffer_load_dwordx4 v37, s[48:51], s63 offen lds\n\t" \
    "v_mfma_f32_16x16x32_bf16 v[172:175], v[60:63], v[76:79], v[172:175]\n\t" \
    "ds_read_b64_tr_b16 v[58:59], v33 offset:2048\n\t" \
    "s_waitcnt lgkmcnt(6)\n\t" \
    "v_mfma_f32_16x16x32_bf16 v[176:179], v[44:47], v[64:67], v[176:179]\n\t" \
    "ds_read_b64_tr_b16 v[60:61], v33 offset:8192\n\t" \
    "ds_read_b64_tr_b16 v[62:63], v33 offset:10240\n\t" \
    "v_mfma_f32_16x16x32_bf16 v[180:183], v[44:47], v[68:71], v[180:183]\n\t" \
    "s_add_u32 s60, s60, 131072\n\t" \
    "v_mfma_f32_16x16x32_bf16 v[184:187], v[44:47], v[72:75], v[184:187]\n\t" \
    "s_add_u32 s61, s61, s52\n\t" \
    "s_add_u32 s62, s62, s53\n\t" \
    "v_mfma_f32_16x16x32_bf16 v[188:191], v[44:47], v[76:79], v[188:191]\n\t" \
    "s_add_u32 s63, s63, s54\n\t" \
    "s_waitcnt lgkmcnt(6)\n\t" \
    "v_mfma_f32_16x16x32_bf16 v[192:195], v[48:51], v[64:67], v[192:195]\n\t" \
    "v_mfma_f32_16x16x32_bf16 v[196:199], v[48:51], v[68:71], v[196:199]\n\t" \
    "v_mfma_f32_16x16x32_bf16 v[200:203], v[48:51], v[72:75], v[200:203]\n\t" \
    "v_mfma_f32_16x16x32_bf16 v[204:207], v[48:51], v[76:79], v[204:207]\n\t" \
    "s_waitcnt vmcnt(10) lgkmcnt(0)\n\t" \
    "s_barrier\n\t" \
    "v_mfma_f32_16x16x32_bf16 v[208:211], v[52:55], v[64:67], v[208:211]\n\t" \
    "ds_read_b128 v[80:83], v34 offset:36864\n\t" \
    "ds_read_b64_tr_b16 v[44:45], v28 offset:36864\n\t" \
    "v_mfma_f32_16x16x32_bf16 v[212:215], v[52:55], v[68:71], v[212:215]\n\t" \
    "ds_read_b64_tr_b16 v[46:47], v28 offset:38912\n\t" \
    "v_mfma_f32_16x16x32_bf16 v[216:219], v[52:55], v[72:75], v[216:219]\n\t" \
    "ds_read_b128 v[84:87], v34 offset:37888\n\t" \
    "ds_read_b128 v[88:91], v34 offset:38912\n\t" \
    "v_mfma_f32_16x16x32_bf16 v[220:223], v[52:55], v[76:79], v[220:223]\n\t" \
    "ds_read_b128 v[92:95], v34 offset:39936\n\t" \
    "v_mfma_f32_16x16x32_bf16 v[224:227], v[56:59], v[64:67], v[224:227]\n\t" \
    "ds_read_b64_tr_b16 v[48:49], v29 offset:36864\n\t" \
    "v_mfma_f32_16x16x32_bf16 v[228:231], v[56:59], v[68:71], v[228:231]\n\t" \
    "ds_read_b64_tr_b16 v[50:51], v29 offset:38912\n\t" \
    "v_mfma_f32_16x16x32_bf16 v[232:235], v[56:59], v[72:75], v[232:235]\n\t" \
    "ds_read_b64_tr_b16 v[52:53], v30 offset:36864\n\t" \
    "v_mfma_f32_16x16x32_bf16 v[236:239], v[56:59], v[76:79], v[236:239]\n\t" \
    "ds_read_b64_tr_b16 v[54:55], v30 offset:38912\n\t" \
    "v_mfma_f32_16x16x32_bf16 v[240:243], v[60:63], v[64:67], v[240:243]\n\t" \
    "ds_read_b64_tr_b16 v[56:57], v30 offset:45056\n\t" \
    "v_mfma_f32_16x16x32_bf16 v[244:247], v[60:63], v[68:71], v[244:247]\n\t" \
    "v_mfma_f32_16x16x32_bf16 v[248:251], v[60:63], v[72:75], v[248:251]\n\t" \
    "ds_read_b64_tr_b16 v[58:59], v30 offset:47104\n\t" \
    "v_mfma_f32_16x16x32_bf16 v[252:255], v[60:63], v[76:79], v[252:255]\n\t" \
    "s_cmp_eq_u32 s64, 1\n\t" \
    "s_cbranch_scc0 LK2PF3_v0_%=\n\t" \
    "s_sub_u32 s55, s55, 73728\n\t" \
    "s_sub_u32 s56, s56, 73728\n\t" \
    "s_sub_u32 s57, s57, 73728\n\t" \
    "s_sub_u32 s58, s58, 73728\n\t" \
    "LK2PF3_v0_%=:\n\t" \
    "s_waitcnt lgkmcnt(6)\n\t" \
    "v_mfma_f32_16x16x32_bf16 v[96:99], v[44:47], v[80:83], v[96:99]\n\t" \
    "s_add_u32 m0, s58, 73728\n\t" \
    "v_mfma_f32_16x16x32_bf16 v[100:103], v[44:47], v[84:87], v[100:103]\n\t" \
    "ds_read_b64_tr_b16 v[60:61], v31 offset:36864\n\t" \
    "v_mfma_f32_16x16x32_bf16 v[104:107], v[44:47], v[88:91], v[104:107]\n\t" \
    "buffer_load_dwordx4 v38, s[36:39], s60 offen lds\n\t" \
    "v_mfma_f32_16x16x32_bf16 v[108:111], v[44:47], v[92:95], v[108:111]\n\t" \
    "ds_read_b64_tr_b16 v[62:63], v31 offset:38912\n\t" \
    "s_waitcnt lgkmcnt(6)\n\t" \
    "v_mfma_f32_16x16x32_bf16 v[112:115], v[48:51], v[80:83], v[112:115]\n\t" \
    "ds_read_b64_tr_b16 v[44:45], v31 offset:45056\n\t" \
    "v_mfma_f32_16x16x32_bf16 v[116:119], v[48:51], v[84:87], v[116:119]\n\t" \
    "buffer_load_dwordx4 v38, s[36:39], s60 offen offset:1024 lds\n\t" \
    "v_mfma_f32_16x16x32_bf16 v[120:123], v[48:51], v[88:91], v[120:123]\n\t" \
    "ds_read_b64_tr_b16 v[46:47], v31 offset:47104\n\t" \
    "v_mfma_f32_16x16x32_bf16 v[124:127], v[48:51], v[92:95], v[124:127]\n\t" \
    "s_waitcnt lgkmcnt(6)\n\t" \
    "v_mfma_f32_16x16x32_bf16 v[128:131], v[52:55], v[80:83], v[128:131]\n\t" \
    "s_add_u32 m0, s55, 73728\n\t" \
    "v_mfma_f32_16x16x32_bf16 v[132:135], v[52:55], v[84:87], v[132:135]\n\t" \
    "ds_read_b64_tr_b16 v[48:49], v32 offset:36864\n\t" \
    "v_mfma_f32_16x16x32_bf16 v[136:139], v[52:55], v[88:91], v[136:139]\n\t" \
    "buffer_load_dwordx4 v35, s[40:43], s61 offen lds\n\t" \
    "v_mfma_f32_16x16x32_bf16 v[140:143], v[52:55], v[92:95], v[140:143]\n\t" \
    "ds_read_b64_tr_b16 v[50:51], v32 offset:38912\n\t" \
    "s_waitcnt lgkmcnt(6)\n\t" \
    "v_mfma_f32_16x16x32_bf16 v[144:147], v[56:59], v[80:83], v[144:147]\n\t" \
    "s_add_u32 m0, s56, 73728\n\t" \
    "v_mfma_f32_16x16x32_bf16 v[148:151], v[56:59], v[84:87], v[148:151]\n\t" \
    "ds_read_b64_tr_b16 v[52:53], v32 offset:45056\n\t" \
    "v_mfma_f32_16x16x32_bf16 v[152:155], v[56:59], v[88:91], v[152:155]\n\t" \
    "buffer_load_dwordx4 v36, s[44:47], s62 offen lds\n\t" \
    "v_mfma_f32_16x16x32_bf16 v[156:159], v[56:59], v[92:95], v[156:159]\n\t" \
    "ds_read_b64_tr_b16 v[54:55], v32 offset:47104\n\t" \
    "s_waitcnt lgkmcnt(6)\n\t" \
    "v_mfma_f32_16x16x32_bf16 v[160:163], v[60:63], v[80:83], v[160:163]\n\t" \
    "s_add_u32 m0, s57, 73728\n\t" \
    "v_mfma_f32_16x16x32_bf16 v[164:167], v[60:63], v[84:87], v[164:167]\n\t" \
    "ds_read_b64_tr_b16 v[56:57], v33 offset:36864\n\t" \
    "v_mfma_f32_16x16x32_bf16 v[168:171], v[60:63], v[88:91], v[168:171]\n\t" \
    "buffer_load_dwordx4 v37, s[48:51], s63 offen lds\n\t" \
    "v_mfma_f32_16x16x32_bf16 v[172:175], v[60:63], v[92:95], v[172:175]\n\t" \
    "ds_read_b64_tr_b16 v[58:59], v33 offset:38912\n\t" \
    "s_waitcnt lgkmcnt(6)\n\t" \
    "v_mfma_f32_16x16x32_bf16 v[176:179], v[44:47], v[80:83], v[176:179]\n\t" \
    "ds_read_b64_tr_b16 v[60:61], v33 offset:45056\n\t" \
    "ds_read_b64_tr_b16 v[62:63], v33 offset:47104\n\t" \
    "v_mfma_f32_16x16x32_bf16 v[180:183], v[44:47], v[84:87], v[180:183]\n\t" \
    "s_add_u32 s60, s60, 131072\n\t" \
    "v_mfma_f32_16x16x32_bf16 v[184:187], v[44:47], v[88:91], v[184:187]\n\t" \
    "s_add_u32 s61, s61, s52\n\t" \
    "s_add_u32 s62, s62, s53\n\t" \
    "v_mfma_f32_16x16x32_bf16 v[188:191], v[44:47], v[92:95], v[188:191]\n\t" \
    "s_add_u32 s63, s63, s54\n\t" \
    "s_waitcnt lgkmcnt(6)\n\t" \
    "v_mfma_f32_16x16x32_bf16 v[192:195], v[48:51], v[80:83], v[192:195]\n\t" \
    "v_subrev_u32 v28, 0x12000, v28\n\t" \
    "v_subrev_u32 v29, 0x12000, v29\n\t" \
    "v_mfma_f32_16x16x32_bf16 v[196:199], v[48:51], v[84:87], v[196:199]\n\t" \
    "v_subrev_u32 v30, 0x12000, v30\n\t" \
    "v_subrev_u32 v31, 0x12000, v31\n\t" \
    "v_mfma_f32_16x16x32_bf16 v[200:203], v[48:51], v[88:91], v[200:203]\n\t" \
    "v_subrev_u32 v32, 0x12000, v32\n\t" \
    "v_subrev_u32 v33, 0x12000, v33\n\t" \
    "v_mfma_f32_16x16x32_bf16 v[204:207], v[48:51], v[92:95], v[204:207]\n\t" \
    "v_subrev_u32 v34, 0x12000, v34\n\t" \
    "s_waitcnt vmcnt(10) lgkmcnt(0)\n\t" \
    "s_barrier\n\t" \
    "v_mfma_f32_16x16x32_bf16 v[208:211], v[52:55], v[80:83], v[208:211]\n\t" \
    "ds_read_b128 v[64:67], v34 offset:0\n\t" \
    "ds_read_b64_tr_b16 v[44:45], v28 offset:0\n\t" \
    "v_mfma_f32_16x16x32_bf16 v[212:215], v[52:55], v[84:87], v[212:215]\n\t" \
    "ds_read_b64_tr_b16 v[46:47], v28 offset:2048\n\t" \
    "v_mfma_f32_16x16x32_bf16 v[216:219], v[52:55], v[88:91], v[216:219]\n\t" \
    "ds_read_b128 v[68:71], v34 offset:1024\n\t" \
    "ds_read_b128 v[72:75], v34 offset:2048\n\t" \
    "v_mfma_f32_16x16x32_bf16 v[220:223], v[52:55], v[92:95], v[220:223]\n\t" \
    "ds_read_b128 v[76:79], v34 offset:3072\n\t" \
    "v_mfma_f32_16x16x32_bf16 v[224:227], v[56:59], v[80:83], v[224:227]\n\t" \
    "ds_read_b64_tr_b16 v[48:49], v29 offset:0\n\t" \
    "v_mfma_f32_16x16x32_bf16 v[228:231], v[56:59], v[84:87], v[228:231]\n\t" \
    "ds_read_b64_tr_b16 v[50:51], v29 offset:2048\n\t" \
    "v_mfma_f32_16x16x32_bf16 v[232:235], v[56:59], v[88:91], v[232:235]\n\t" \
    "ds_read_b64_tr_b16 v[52:53], v30 offset:0\n\t" \
    "v_mfma_f32_16x16x32_bf16 v[236:239], v[56:59], v[92:95], v[236:239]\n\t" \
    "ds_read_b64_tr_b16 v[54:55], v30 offset:2048\n\t" \
    "v_mfma_f32_16x16x32_bf16 v[240:243], v[60:63], v[80:83], v[240:243]\n\t" \
    "ds_read_b64_tr_b16 v[56:57], v30 offset:8192\n\t" \
    "v_mfma_f32_16x16x32_bf16 v[244:247], v[60:63], v[84:87], v[244:247]\n\t" \
    "v_mfma_f32_16x16x32_bf16 v[248:251], v[60:63], v[88:91], v[248:251]\n\t" \
    "ds_read_b64_tr_b16 v[58:59], v30 offset:10240\n\t" \
    "v_mfma_f32_16x16x32_bf16 v[252:255], v[60:63], v[92:95], v[252:255]\n\t" \
    "s_sub_u32 s64, s64, 1\n\t" \
    "s_cmp_lg_u32 s64, 0\n\t" \
    "s_cbranch_scc1 LK2_v0_%=\n\t" \
    "s_branch LK2END_%=\n\t" \
    "LK2V1_%=:\n\t" \
    "s_nop 4\n\t" \
    "s_mov_b32 s60, 0\n\t" \
    "s_mov_b32 s61, 0\n\t" \
    "s_mov_b32 s62, 0\n\t" \
    "s_mul_i32 s63, s54, 1\n\t" \
    "s_add_u32 m0, s58, 0\n\t" \
    "s_nop 0\n\t" \
    "buffer_load_dwordx4 v38, s[36:39], s60 offen lds\n\t" \
    "v_mov_b32 v96, 0\n\t" \
    "v_mov_b32 v97, 0\n\t" \
    "v_mov_b32 v98, 0\n\t" \
    "v_mov_b32 v99, 0\n\t" \
    "v_mov_b32 v100, 0\n\t" \
    "v_mov_b32 v101, 0\n\t" \
    "v_mov_b32 v102, 0\n\t" \
    "v_mov_b32 v103, 0\n\t" \
    "v_mov_b32 v104, 0\n\t" \
    "v_mov_b32 v105, 0\n\t" \
    "v_mov_b32 v106, 0\n\t" \
    "v_mov_b32 v107, 0\n\t" \
    "v_mov_b32 v108, 0\n\t" \
    "v_mov_b32 v109, 0\n\t" \
    "v_mov_b32 v110, 0\n\t" \
    "v_mov_b32 v111, 0\n\t" \
    "buffer_load_dwordx4 v38, s[36:39], s60 offen offset:1024 lds\n\t" \
    "v_mov_b32 v112, 0\n\t" \
    "v_mov_b32 v113, 0\n\t" \
    "v_mov_b32 v114, 0\n\t" \
    "v_mov_b32 v115, 0\n\t" \
    "v_mov_b32 v116, 0\n\t" \
    "v_mov_b32 v117, 0\n\t" \
    "v_mov_b32 v118, 0\n\t" \
    "v_mov_b32 v119, 0\n\t" \
    "v_mov_b32 v120, 0\n\t" \
    "v_mov_b32 v121, 0\n\t" \
    "v_mov_b32 v122, 0\n\t" \
    "v_mov_b32 v123, 0\n\t" \
    "v_mov_b32 v124, 0\n\t" \
    "v_mov_b32 v125, 0\n\t" \
    "v_mov_b32 v126, 0\n\t" \
    "v_mov_b32 v127, 0\n\t" \
    "s_add_u32 m0, s55, 0\n\t" \
    "s_nop 0\n\t" \
    "buffer_load_dwordx4 v35, s[40:43], s61 offen lds\n\t" \
    "v_mov_b32 v128, 0\n\t" \
    "v_mov_b32 v129, 0\n\t" \
    "v_mov_b32 v130, 0\n\t" \
    "v_mov_b32 v131, 0\n\t" \
    "v_mov_b32 v132, 0\n\t" \
    "v_mov_b32 v133, 0\n\t" \
    "v_mov_b32 v134, 0\n\t" \
    "v_mov_b32 v135, 0\n\t" \
    "v_mov_b32 v136, 0\n\t" \
    "v_mov_b32 v137, 0\n\t" \
    "v_mov_b32 v138, 0\n\t" \
    "v_mov_b32 v139, 0\n\t" \
    "v_mov_b32 v140, 0\n\t" \
    "v_mov_b32 v141, 0\n\t" \
    "v_mov_b32 v142, 0\n\t" \
    "v_mov_b32 v143, 0\n\t" \
    "s_add_u32 m0, s56, 0\n\t" \
    "s_nop 0\n\t" \
    "buffer_load_dwordx4 v36, s[44:47], s62 offen lds\n\t" \
    "v_mov_b32 v144, 0\n\t" \
    "v_mov_b32 v145, 0\n\t" \
    "v_mov_b32 v146, 0\n\t" \
    "v_mov_b32 v147, 0\n\t" \
    "v_mov_b32 v148, 0\n\t" \
    "v_mov_b32 v149, 0\n\t" \
    "v_mov_b32 v150, 0\n\t" \
    "v_mov_b32 v151, 0\n\t" \
    "v_mov_b32 v152, 0\n\t" \
    "v_mov_b32 v153, 0\n\t" \
    "v_mov_b32 v154, 0\n\t" \
    "v_mov_b32 v155, 0\n\t" \
    "v_mov_b32 v156, 0\n\t" \
    "v_mov_b32 v157, 0\n\t" \
    "v_mov_b32 v158, 0\n\t" \
    "v_mov_b32 v159, 0\n\t" \
    "s_add_u32 m0, s57, 0\n\t" \
    "s_nop 0\n\t" \
    "buffer_load_dwordx4 v37, s[48:51], s63 offen lds\n\t" \
    "v_mov_b32 v160, 0\n\t" \
    "v_mov_b32 v161, 0\n\t" \
    "v_mov_b32 v162, 0\n\t" \
    "v_mov_b32 v163, 0\n\t" \
    "v_mov_b32 v164, 0\n\t" \
    "v_mov_b32 v165, 0\n\t" \
    "v_mov_b32 v166, 0\n\t" \
    "v_mov_b32 v167, 0\n\t" \
    "v_mov_b32 v168, 0\n\t" \
    "v_mov_b32 v169, 0\n\t" \
    "v_mov_b32 v170, 0\n\t" \
    "v_mov_b32 v171, 0\n\t" \
    "v_mov_b32 v172, 0\n\t" \
    "v_mov_b32 v173, 0\n\t" \
    "v_mov_b32 v174, 0\n\t" \
    "v_mov_b32 v175, 0\n\t" \
    "s_add_u32 s60, s60, 131072\n\t" \
    "s_add_u32 s61, s61, s52\n\t" \
    "s_add_u32 s62, s62, s53\n\t" \
    "s_add_u32 s63, s63, s54\n\t" \
    "s_add_u32 m0, s58, 36864\n\t" \
    "s_nop 0\n\t" \
    "buffer_load_dwordx4 v38, s[36:39], s60 offen lds\n\t" \
    "v_mov_b32 v176, 0\n\t" \
    "v_mov_b32 v177, 0\n\t" \
    "v_mov_b32 v178, 0\n\t" \
    "v_mov_b32 v179, 0\n\t" \
    "v_mov_b32 v180, 0\n\t" \
    "v_mov_b32 v181, 0\n\t" \
    "v_mov_b32 v182, 0\n\t" \
    "v_mov_b32 v183, 0\n\t" \
    "v_mov_b32 v184, 0\n\t" \
    "v_mov_b32 v185, 0\n\t" \
    "v_mov_b32 v186, 0\n\t" \
    "v_mov_b32 v187, 0\n\t" \
    "v_mov_b32 v188, 0\n\t" \
    "v_mov_b32 v189, 0\n\t" \
    "v_mov_b32 v190, 0\n\t" \
    "v_mov_b32 v191, 0\n\t" \
    "buffer_load_dwordx4 v38, s[36:39], s60 offen offset:1024 lds\n\t" \
    "v_mov_b32 v192, 0\n\t" \
    "v_mov_b32 v193, 0\n\t" \
    "v_mov_b32 v194, 0\n\t" \
    "v_mov_b32 v195, 0\n\t" \
    "v_mov_b32 v196, 0\n\t" \
    "v_mov_b32 v197, 0\n\t" \
    "v_mov_b32 v198, 0\n\t" \
    "v_mov_b32 v199, 0\n\t" \
    "v_mov_b32 v200, 0\n\t" \
    "v_mov_b32 v201, 0\n\t" \
    "v_mov_b32 v202, 0\n\t" \
    "v_mov_b32 v203, 0\n\t" \
    "v_mov_b32 v204, 0\n\t" \
    "v_mov_b32 v205, 0\n\t" \
    "v_mov_b32 v206, 0\n\t" \
    "v_mov_b32 v207, 0\n\t" \
    "s_add_u32 m0, s55, 36864\n\t" \
    "s_nop 0\n\t" \
    "buffer_load_dwordx4 v35, s[40:43], s61 offen lds\n\t" \
    "v_mov_b32 v208, 0\n\t" \
    "v_mov_b32 v209, 0\n\t" \
    "v_mov_b32 v210, 0\n\t" \
    "v_mov_b32 v211, 0\n\t" \
    "v_mov_b32 v212, 0\n\t" \
    "v_mov_b32 v213, 0\n\t" \
    "v_mov_b32 v214, 0\n\t" \
    "v_mov_b32 v215, 0\n\t" \
    "v_mov_b32 v216, 0\n\t" \
    "v_mov_b32 v217, 0\n\t" \
    "v_mov_b32 v218, 0\n\t" \
    "v_mov_b32 v219, 0\n\t" \
    "v_mov_b32 v220, 0\n\t" \
    "v_mov_b32 v221, 0\n\t" \
    "v_mov_b32 v222, 0\n\t" \
    "v_mov_b32 v223, 0\n\t" \
    "s_add_u32 m0, s56, 36864\n\t" \
    "s_nop 0\n\t" \
    "buffer_load_dwordx4 v36, s[44:47], s62 offen lds\n\t" \
    "v_mov_b32 v224, 0\n\t" \
    "v_mov_b32 v225, 0\n\t" \
    "v_mov_b32 v226, 0\n\t" \
    "v_mov_b32 v227, 0\n\t" \
    "v_mov_b32 v228, 0\n\t" \
    "v_mov_b32 v229, 0\n\t" \
    "v_mov_b32 v230, 0\n\t" \
    "v_mov_b32 v231, 0\n\t" \
    "v_mov_b32 v232, 0\n\t" \
    "v_mov_b32 v233, 0\n\t" \
    "v_mov_b32 v234, 0\n\t" \
    "v_mov_b32 v235, 0\n\t" \
    "v_mov_b32 v236, 0\n\t" \
    "v_mov_b32 v237, 0\n\t" \
    "v_mov_b32 v238, 0\n\t" \
    "v_mov_b32 v239, 0\n\t" \
    "s_add_u32 m0, s57, 0\n\t" \
    "s_nop 0\n\t" \
    "buffer_load_dwordx4 v37, s[48:51], s63 offen lds\n\t" \
    "v_mov_b32 v240, 0\n\t" \
    "v_mov_b32 v241, 0\n\t" \
    "v_mov_b32 v242, 0\n\t" \
    "v_mov_b32 v243, 0\n\t" \
    "v_mov_b32 v244, 0\n\t" \
    "v_mov_b32 v245, 0\n\t" \
    "v_mov_b32 v246, 0\n\t" \
    "v_mov_b32 v247, 0\n\t" \
    "v_mov_b32 v248, 0\n\t" \
    "v_mov_b32 v249, 0\n\t" \
    "v_mov_b32 v250, 0\n\t" \
    "v_mov_b32 v251, 0\n\t" \
    "v_mov_b32 v252, 0\n\t" \
    "v_mov_b32 v253, 0\n\t" \
    "v_mov_b32 v254, 0\n\t" \
    "v_mov_b32 v255, 0\n\t" \
    "s_add_u32 s60, s60, 131072\n\t" \
    "s_add_u32 s61, s61, s52\n\t" \
    "s_add_u32 s62, s62, s53\n\t" \
    "s_add_u32 s63, s63, s54\n\t" \
    "s_mov_b32 s64, 8\n\t" \
    "s_waitcnt vmcnt(5)\n\t" \
    "s_barrier\n\t" \
    "ds_read_b128 v[64:67], v34 offset:0\n\t" \
    "ds_read_b128 v[68:71], v34 offset:1024\n\t" \
    "ds_read_b128 v[72:75], v34 offset:2048\n\t" \
    "ds_read_b128 v[76:79], v34 offset:3072\n\t" \
    "ds_read_b64_tr_b16 v[44:45], v28 offset:0\n\t" \
    "ds_read_b64_tr_b16 v[46:47], v28 offset:2048\n\t" \
    "ds_read_b64_tr_b16 v[48:49], v29 offset:0\n\t" \
    "ds_read_b64_tr_b16 v[50:51], v29 offset:2048\n\t" \
    "ds_read_b64_tr_b16 v[52:53], v30 offset:0\n\t" \
    "ds_read_b64_tr_b16 v[54:55], v30 offset:2048\n\t" \
    "ds_read_b64_tr_b16 v[56:57], v30 offset:8192\n\t" \
    "ds_read_b64_tr_b16 v[58:59], v30 offset:10240\n\t" \
    "s_add_u32 m0, s58, 73728\n\t" \
    "s_nop 0\n\t" \
    "buffer_load_dwordx4 v38, s[36:39], s60 offen lds\n\t" \
    "buffer_load_dwordx4 v38, s[36:39], s60 offen offset:1024 lds\n\t" \
    "s_add_u32 m0, s55, 73728\n\t" \
    "s_nop 0\n\t" \
    "buffer_load_dwordx4 v35, s[40:43], s61 offen lds\n\t" \
    "s_add_u32 m0, s56, 73728\n\t" \
    "s_nop 0\n\t" \
    "buffer_load_dwordx4 v36, s[44:47], s62 offen lds\n\t" \
    "s_add_u32 m0, s57, 0\n\t" \
    "s_nop 0\n\t" \
    "buffer_load_dwordx4 v37, s[48:51], s63 offen lds\n\t" \
    "s_add_u32 s60, s60, 131072\n\t" \
    "s_add_u32 s61, s61, s52\n\t" \
    "s_add_u32 s62, s62, s53\n\t" \
    "s_add_u32 s63, s63, s54\n\t" \
    "s_cmp_eq_u32 s73, 0\n\t" \
    "s_cbranch_scc1 LK2NP_v1_%=\n\t" \
    "s_add_u32 m0, s72, 155648\n\t" \
    "s_nop 0\n\t" \
    "global_load_lds_dword v[24:25], off\n\t" \
    "LK2NP_v1_%=:\n\t" \
    "LK2_v1_%=:\n\t" \
    "s_waitcnt lgkmcnt(6)\n\t" \
    "v_mfma_f32_16x16x32_bf16 v[96:99], v[44:47], v[64:67], v[96:99]\n\t" \
    "s_add_u32 m0, s58, 110592\n\t" \
    "v_mfma_f32_16x16x32_bf16 v[100:103], v[44:47], v[68:71], v[100:103]\n\t" \
    "ds_read_b64_tr_b16 v[60:61], v31 offset:0\n\t" \
    "v_mfma_f32_16x16x32_bf16 v[104:107], v[44:47], v[72:75], v[104:107]\n\t" \
    "buffer_load_dwordx4 v38, s[36:39], s60 offen lds\n\t" \
    "v_mfma_f32_16x16x32_bf16 v[108:111], v[44:47], v[76:79], v[108:111]\n\t" \
    "ds_read_b64_tr_b16 v[62:63], v31 offset:2048\n\t" \
    "s_waitcnt lgkmcnt(6)\n\t" \
    "v_mfma_f32_16x16x32_bf16 v[112:115], v[48:51], v[64:67], v[112:115]\n\t" \
    "ds_read_b64_tr_b16 v[44:45], v31 offset:8192\n\t" \
    "v_mfma_f32_16x16x32_bf16 v[116:119], v[48:51], v[68:71], v[116:119]\n\t" \
    "buffer_load_dwordx4 v38, s[36:39], s60 offen offset:1024 lds\n\t" \
    "v_mfma_f32_16x16x32_bf16 v[120:123], v[48:51], v[72:75], v[120:123]\n\t" \
    "ds_read_b64_tr_b16 v[46:47], v31 offset:10240\n\t" \
    "v_mfma_f32_16x16x32_bf16 v[124:127], v[48:51], v[76:79], v[124:127]\n\t" \
    "s_waitcnt lgkmcnt(6)\n\t" \
    "v_mfma_f32_16x16x32_bf16 v[128:131], v[52:55], v[64:67], v[128:131]\n\t" \
    "s_add_u32 m0, s55, 110592\n\t" \
    "v_mfma_f32_16x16x32_bf16 v[132:135], v[52:55], v[68:71], v[132:135]\n\t" \
    "ds_read_b64_tr_b16 v[48:49], v32 offset:0\n\t" \
    "v_mfma_f32_16x16x32_bf16 v[136:139], v[52:55], v[72:75], v[136:139]\n\t" \
    "buffer_load_dwordx4 v35, s[40:43], s61 offen lds\n\t" \
    "v_mfma_f32_16x16x32_bf16 v[140:143], v[52:55], v[76:79], v[140:143]\n\t" \
    "ds_read_b64_tr_b16 v[50:51], v32 offset:2048\n\t" \
    "s_waitcnt lgkmcnt(6)\n\t" \
    "v_mfma_f32_16x16x32_bf16 v[144:147], v[56:59], v[64:67], v[144:147]\n\t" \
    "s_add_u32 m0, s56, 110592\n\t" \
    "v_mfma_f32_16x16x32_bf16 v[148:151], v[56:59], v[68:71], v[148:151]\n\t" \
    "ds_read_b64_tr_b16 v[52:53], v32 offset:8192\n\t" \
    "v_mfma_f32_16x16x32_bf16 v[152:155], v[56:59], v[72:75], v[152:155]\n\t" \
    "buffer_load_dwordx4 v36, s[44:47], s62 offen lds\n\t" \
    "v_mfma_f32_16x16x32_bf16 v[156:159], v[56:59], v[76:79], v[156:159]\n\t" \
    "ds_read_b64_tr_b16 v[54:55], v32 offset:10240\n\t" \
    "s_waitcnt lgkmcnt(6)\n\t" \
    "v_mfma_f32_16x16x32_bf16 v[160:163], v[60:63], v[64:67], v[160:163]\n\t" \
    "s_add_u32 m0, s57, 0\n\t" \
    "v_mfma_f32_16x16x32_bf16 v[164:167], v[60:63], v[68:71], v[164:167]\n\t" \
    "ds_read_b64_tr_b16 v[56:57], v33 offset:0\n\t" \
    "v_mfma_f32_16x16x32_bf16 v[168:171], v[60:63], v[72:75], v[168:171]\n\t" \
    "buffer_load_dwordx4 v37, s[48:51], s63 offen lds\n\t" \
    "v_mfma_f32_16x16x32_bf16 v[172:175], v[60:63], v[76:79], v[172:175]\n\t" \
    "ds_read_b64_tr_b16 v[58:59], v33 offset:2048\n\t" \
    "s_waitcnt lgkmcnt(6)\n\t" \
    "v_mfma_f32_16x16x32_bf16 v[176:179], v[44:47], v[64:67], v[176:179]\n\t" \
    "ds_read_b64_tr_b16 v[60:61], v33 offset:8192\n\t" \
    "ds_read_b64_tr_b16 v[62:63], v33 offset:10240\n\t" \
    "v_mfma_f32_16x16x32_bf16 v[180:183], v[44:47], v[68:71], v[180:183]\n\t" \
    "s_add_u32 s60, s60, 131072\n\t" \
    "v_mfma_f32_16x16x32_bf16 v[184:187], v[44:47], v[72:75], v[184:187]\n\t" \
    "s_add_u32 s61, s61, s52\n\t" \
    "s_add_u32 s62, s62, s53\n\t" \
    "v_mfma_f32_16x16x32_bf16 v[188:191], v[44:47], v[76:79], v[188:191]\n\t" \
    "s_add_u32 s63, s63, s54\n\t" \
    "s_waitcnt lgkmcnt(6)\n\t" \
    "v_mfma_f32_16x16x32_bf16 v[192:195], v[48:51], v[64:67], v[192:195]\n\t" \
    "v_mfma_f32_16x16x32_bf16 v[196:199], v[48:51], v[68:71], v[196:199]\n\t" \
    "v_mfma_f32_16x16x32_bf16 v[200:203], v[48:51], v[72:75], v[200:203]\n\t" \
    "v_mfma_f32_16x16x32_bf16 v[204:207], v[48:51], v[76:79], v[204:207]\n\t" \
    "s_waitcnt vmcnt(10) lgkmcnt(0)\n\t" \
    "s_barrier\n\t" \
    "v_mfma_f32_16x16x32_bf16 v[208:211], v[52:55], v[64:67], v[208:211]\n\t" \
    "ds_read_b128 v[80:83], v34 offset:36864\n\t" \
    "ds_read_b64_tr_b16 v[44:45], v28 offset:36864\n\t" \
    "v_mfma_f32_16x16x32_bf16 v[212:215], v[52:55], v[68:71], v[212:215]\n\t" \
    "ds_read_b64_tr_b16 v[46:47], v28 offset:38912\n\t" \
    "v_mfma_f32_16x16x32_bf16 v[216:219], v[52:55], v[72:75], v[216:219]\n\t" \
    "ds_read_b128 v[84:87], v34 offset:37888\n\t" \
    "ds_read_b128 v[88:91], v34 offset:38912\n\t" \
    "v_mfma_f32_16x16x32_bf16 v[220:223], v[52:55], v[76:79], v[220:223]\n\t" \
    "ds_read_b128 v[92:95], v34 offset:39936\n\t" \
    "v_mfma_f32_16x16x32_bf16 v[224:227], v[56:59], v[64:67], v[224:227]\n\t" \
    "ds_read_b64_tr_b16 v[48:49], v29 offset:36864\n\t" \
    "v_mfma_f32_16x16x32_bf16 v[228:231], v[56:59], v[68:71], v[228:231]\n\t" \
    "ds_read_b64_tr_b16 v[50:51], v29 offset:38912\n\t" \
    "v_mfma_f32_16x16x32_bf16 v[232:235], v[56:59], v[72:75], v[232:235]\n\t" \
    "ds_read_b64_tr_b16 v[52:53], v30 offset:36864\n\t" \
    "v_mfma_f32_16x16x32_bf16 v[236:239], v[56:59], v[76:79], v[236:239]\n\t" \
    "ds_read_b64_tr_b16 v[54:55], v30 offset:38912\n\t" \
    "v_mfma_f32_16x16x32_bf16 v[240:243], v[60:63], v[64:67], v[240:243]\n\t" \
    "ds_read_b64_tr_b16 v[56:57], v30 offset:45056\n\t" \
    "v_mfma_f32_16x16x32_bf16 v[244:247], v[60:63], v[68:71], v[244:247]\n\t" \
    "v_mfma_f32_16x16x32_bf16 v[248:251], v[60:63], v[72:75], v[248:251]\n\t" \
    "ds_read_b64_tr_b16 v[58:59], v30 offset:47104\n\t" \
    "v_mfma_f32_16x16x32_bf16 v[252:255], v[60:63], v[76:79], v[252:255]\n\t" \
    "s_cmp_eq_u32 s64, 1\n\t" \
    "s_cbranch_scc0 LK2PF1_v1_%=\n\t" \
    "s_mov_b32 s40, s68\n\t" \
    "s_mov_b32 s41, s69\n\t" \
    "s_mov_b32 s42, 7340032\n\t" \
    "s_mov_b32 s52, 4194304\n\t" \
    "s_mov_b32 s61, 0\n\t" \
    "v_mov_b32 v35, v39\n\t" \
    "s_mov_b32 s55, s72\n\t" \
    "s_mov_b32 s44, s68\n\t" \
    "s_mov_b32 s45, s69\n\t" \
    "s_mov_b32 s46, 7340032\n\t" \
    "s_mov_b32 s53, 4194304\n\t" \
    "s_mov_b32 s62, 2097152\n\t" \
    "v_mov_b32 v36, v39\n\t" \
    "s_mov_b32 s56, s72\n\t" \
    "s_mov_b32 s50, 0\n\t" \
    "s_mov_b32 s58, s72\n\t" \
    "LK2PF1_v1_%=:\n\t" \
    "s_waitcnt lgkmcnt(6)\n\t" \
    "v_mfma_f32_16x16x32_bf16 v[96:99], v[44:47], v[80:83], v[96:99]\n\t" \
    "s_add_u32 m0, s58, 0\n\t" \
    "v_mfma_f32_16x16x32_bf16 v[100:103], v[44:47], v[84:87], v[100:103]\n\t" \
    "ds_read_b64_tr_b16 v[60:61], v31 offset:36864\n\t" \
    "v_mfma_f32_16x16x32_bf16 v[104:107], v[44:47], v[88:91], v[104:107]\n\t" \
    "buffer_load_dwordx4 v38, s[36:39], s60 offen lds\n\t" \
    "v_mfma_f32_16x16x32_bf16 v[108:111], v[44:47], v[92:95], v[108:111]\n\t" \
    "ds_read_b64_tr_b16 v[62:63], v31 offset:38912\n\t" \
    "s_waitcnt lgkmcnt(6)\n\t" \
    "v_mfma_f32_16x16x32_bf16 v[112:115], v[48:51], v[80:83], v[112:115]\n\t" \
    "ds_read_b64_tr_b16 v[44:45], v31 offset:45056\n\t" \
    "v_mfma_f32_16x16x32_bf16 v[116:119], v[48:51], v[84:87], v[116:119]\n\t" \
    "buffer_load_dwordx4 v38, s[36:39], s60 offen offset:1024 lds\n\t" \
    "v_mfma_f32_16x16x32_bf16 v[120:123], v[48:51], v[88:91], v[120:123]\n\t" \
    "ds_read_b64_tr_b16 v[46:47], v31 offset:47104\n\t" \
    "v_mfma_f32_16x16x32_bf16 v[124:127], v[48:51], v[92:95], v[124:127]\n\t" \
    "s_waitcnt lgkmcnt(6)\n\t" \
    "v_mfma_f32_16x16x32_bf16 v[128:131], v[52:55], v[80:83], v[128:131]\n\t" \
    "s_add_u32 m0, s55, 0\n\t" \
    "v_mfma_f32_16x16x32_bf16 v[132:135], v[52:55], v[84:87], v[132:135]\n\t" \
    "ds_read_b64_tr_b16 v[48:49], v32 offset:36864\n\t" \
    "v_mfma_f32_16x16x32_bf16 v[136:139], v[52:55], v[88:91], v[136:139]\n\t" \
    "buffer_load_dwordx4 v35, s[40:43], s61 offen lds\n\t" \
    "v_mfma_f32_16x16x32_bf16 v[140:143], v[52:55], v[92:95], v[140:143]\n\t" \
    "ds_read_b64_tr_b16 v[50:51], v32 offset:38912\n\t" \
    "s_waitcnt lgkmcnt(6)\n\t" \
    "v_mfma_f32_16x16x32_bf16 v[144:147], v[56:59], v[80:83], v[144:147]\n\t" \
    "s_add_u32 m0, s56, 0\n\t" \
    "v_mfma_f32_16x16x32_bf16 v[148:151], v[56:59], v[84:87], v[148:151]\n\t" \
    "ds_read_b64_tr_b16 v[52:53], v32 offset:45056\n\t" \
    "v_mfma_f32_16x16x32_bf16 v[152:155], v[56:59], v[88:91], v[152:155]\n\t" \
    "buffer_load_dwordx4 v36, s[44:47], s62 offen lds\n\t" \
    "v_mfma_f32_16x16x32_bf16 v[156:159], v[56:59], v[92:95], v[156:159]\n\t" \
    "ds_read_b64_tr_b16 v[54:55], v32 offset:47104\n\t" \
    "s_waitcnt lgkmcnt(6)\n\t" \
    "v_mfma_f32_16x16x32_bf16 v[160:163], v[60:63], v[80:83], v[160:163]\n\t" \
    "s_add_u32 m0, s57, 0\n\t" \
    "v_mfma_f32_16x16x32_bf16 v[164:167], v[60:63], v[84:87], v[164:167]\n\t" \
    "ds_read_b64_tr_b16 v[56:57], v33 offset:36864\n\t" \
    "v_mfma_f32_16x16x32_bf16 v[168:171], v[60:63], v[88:91], v[168:171]\n\t" \
    "buffer_load_dwordx4 v37, s[48:51], s63 offen lds\n\t" \
    "v_mfma_f32_16x16x32_bf16 v[172:175], v[60:63], v[92:95], v[172:175]\n\t" \
    "ds_read_b64_tr_b16 v[58:59], v33 offset:38912\n\t" \
    "s_waitcnt lgkmcnt(6)\n\t" \
    "v_mfma_f32_16x16x32_bf16 v[176:179], v[44:47], v[80:83], v[176:179]\n\t" \
    "ds_read_b64_tr_b16 v[60:61], v33 offset:45056\n\t" \
    "ds_read_b64_tr_b16 v[62:63], v33 offset:47104\n\t" \
    "v_mfma_f32_16x16x32_bf16 v[180:183], v[44:47], v[84:87], v[180:183]\n\t" \
    "s_add_u32 s60, s60, 131072\n\t" \
    "v_mfma_f32_16x16x32_bf16 v[184:187], v[44:47], v[88:91], v[184:187]\n\t" \
    "s_add_u32 s61, s61, s52\n\t" \
    "s_add_u32 s62, s62, s53\n\t" \
    "v_mfma_f32_16x16x32_bf16 v[188:191], v[44:47], v[92:95], v[188:191]\n\t" \
    "s_add_u32 s63, s63, s54\n\t" \
    "s_waitcnt lgkmcnt(6)\n\t" \
    "v_mfma_f32_16x16x32_bf16 v[192:195], v[48:51], v[80:83], v[192:195]\n\t" \
    "v_add_u32 v28, 0x12000, v28\n\t" \
    "v_add_u32 v29, 0x12000, v29\n\t" \
    "v_mfma_f32_16x16x32_bf16 v[196:199], v[48:51], v[84:87], v[196:199]\n\t" \
    "v_add_u32 v30, 0x12000, v30\n\t" \
    "v_add_u32 v31, 0x12000, v31\n\t" \
    "v_mfma_f32_16x16x32_bf16 v[200:203], v[48:51], v[88:91], v[200:203]\n\t" \
    "v_add_u32 v32, 0x12000, v32\n\t" \
    "v_add_u32 v33, 0x12000, v33\n\t" \
    "v_mfma_f32_16x16x32_bf16 v[204:207], v[48:51], v[92:95], v[204:207]\n\t" \
    "v_add_u32 v34, 0x12000, v34\n\t" \
    "s_waitcnt vmcnt(10) lgkmcnt(0)\n\t" \
    "s_barrier\n\t" \
    "v_mfma_f32_16x16x32_bf16 v[208:211], v[52:55], v[80:83], v[208:211]\n\t" \
    "ds_read_b128 v[64:67], v34 offset:0\n\t" \
    "ds_read_b64_tr_b16 v[44:45], v28 offset:0\n\t" \
    "v_mfma_f32_16x16x32_bf16 v[212:215], v[52:55], v[84:87], v[212:215]\n\t" \
    "ds_read_b64_tr_b16 v[46:47], v28 offset:2048\n\t" \
    "v_mfma_f32_16x16x32_bf16 v[216:219], v[52:55], v[88:91], v[216:219]\n\t" \
    "ds_read_b128 v[68:71], v34 offset:1024\n\t" \
    "ds_read_b128 v[72:75], v34 offset:2048\n\t" \
    "v_mfma_f32_16x16x32_bf16 v[220:223], v[52:55], v[92:95], v[220:223]\n\t" \
    "ds_read_b128 v[76:79], v34 offset:3072\n\t" \
    "v_mfma_f32_16x16x32_bf16 v[224:227], v[56:59], v[80:83], v[224:227]\n\t" \
    "ds_read_b64_tr_b16 v[48:49], v29 offset:0\n\t" \
    "v_mfma_f32_16x16x32_bf16 v[228:231], v[56:59], v[84:87], v[228:231]\n\t" \
    "ds_read_b64_tr_b16 v[50:51], v29 offset:2048\n\t" \
    "v_mfma_f32_16x16x32_bf16 v[232:235], v[56:59], v[88:91], v[232:235]\n\t" \
    "ds_read_b64_tr_b16 v[52:53], v30 offset:0\n\t" \
    "v_mfma_f32_16x16x32_bf16 v[236:239], v[56:59], v[92:95], v[236:239]\n\t" \
    "ds_read_b64_tr_b16 v[54:55], v30 offset:2048\n\t" \
    "v_mfma_f32_16x16x32_bf16 v[240:243], v[60:63], v[80:83], v[240:243]\n\t" \
    "ds_read_b64_tr_b16 v[56:57], v30 offset:8192\n\t" \
    "v_mfma_f32_16x16x32_bf16 v[244:247], v[60:63], v[84:87], v[244:247]\n\t" \
    "v_mfma_f32_16x16x32_bf16 v[248:251], v[60:63], v[88:91], v[248:251]\n\t" \
    "ds_read_b64_tr_b16 v[58:59], v30 offset:10240\n\t" \
    "v_mfma_f32_16x16x32_bf16 v[252:255], v[60:63], v[92:95], v[252:255]\n\t" \
    "s_waitcnt lgkmcnt(6)\n\t" \
    "v_mfma_f32_16x16x32_bf16 v[96:99], v[44:47], v[64:67], v[96:99]\n\t" \
    "s_add_u32 m0, s58, 36864\n\t" \
    "v_mfma_f32_16x16x32_bf16 v[100:103], v[44:47], v[68:71], v[100:103]\n\t" \
    "ds_read_b64_tr_b16 v[60:61], v31 offset:0\n\t" \
    "v_mfma_f32_16x16x32_bf16 v[104:107], v[44:47], v[72:75], v[104:107]\n\t" \
    "buffer_load_dwordx4 v38, s[36:39], s60 offen lds\n\t" \
    "v_mfma_f32_16x16x32_bf16 v[108:111], v[44:47], v[76:79], v[108:111]\n\t" \
    "ds_read_b64_tr_b16 v[62:63], v31 offset:2048\n\t" \
    "s_waitcnt lgkmcnt(6)\n\t" \
    "v_mfma_f32_16x16x32_bf16 v[112:115], v[48:51], v[64:67], v[112:115]\n\t" \
    "ds_read_b64_tr_b16 v[44:45], v31 offset:8192\n\t" \
    "v_mfma_f32_16x16x32_bf16 v[116:119], v[48:51], v[68:71], v[116:119]\n\t" \
    "buffer_load_dwordx4 v38, s[36:39], s60 offen offset:1024 lds\n\t" \
    "v_mfma_f32_16x16x32_bf16 v[120:123], v[48:51], v[72:75], v[120:123]\n\t" \
    "ds_read_b64_tr_b16 v[46:47], v31 offset:10240\n\t" \
    "v_mfma_f32_16x16x32_bf16 v[124:127], v[48:51], v[76:79], v[124:127]\n\t" \
    "s_waitcnt lgkmcnt(6)\n\t" \
    "v_mfma_f32_16x16x32_bf16 v[128:131], v[52:55], v[64:67], v[128:131]\n\t" \
    "s_add_u32 m0, s55, 36864\n\t" \
    "v_mfma_f32_16x16x32_bf16 v[132:135], v[52:55], v[68:71], v[132:135]\n\t" \
    "ds_read_b64_tr_b16 v[48:49], v32 offset:0\n\t" \
    "v_mfma_f32_16x16x32_bf16 v[136:139], v[52:55], v[72:75], v[136:139]\n\t" \
    "buffer_load_dwordx4 v35, s[40:43], s61 offen lds\n\t" \
    "v_mfma_f32_16x16x32_bf16 v[140:143], v[52:55], v[76:79], v[140:143]\n\t" \
    "ds_read_b64_tr_b16 v[50:51], v32 offset:2048\n\t" \
    "s_waitcnt lgkmcnt(6)\n\t" \
    "v_mfma_f32_16x16x32_bf16 v[144:147], v[56:59], v[64:67], v[144:147]\n\t" \
    "s_add_u32 m0, s56, 36864\n\t" \
    "v_mfma_f32_16x16x32_bf16 v[148:151], v[56:59], v[68:71], v[148:151]\n\t" \
    "ds_read_b64_tr_b16 v[52:53], v32 offset:8192\n\t" \
    "v_mfma_f32_16x16x32_bf16 v[152:155], v[56:59], v[72:75], v[152:155]\n\t" \
    "buffer_load_dwordx4 v36, s[44:47], s62 offen lds\n\t" \
    "v_mfma_f32_16x16x32_bf16 v[156:159], v[56:59], v[76:79], v[156:159]\n\t" \
    "ds_read_b64_tr_b16 v[54:55], v32 offset:10240\n\t" \
    "s_waitcnt lgkmcnt(6)\n\t" \
    "v_mfma_f32_16x16x32_bf16 v[160:163], v[60:63], v[64:67], v[160:163]\n\t" \
    "s_add_u32 m0, s57, 0\n\t" \
    "v_mfma_f32_16x16x32_bf16 v[164:167], v[60:63], v[68:71], v[164:167]\n\t" \
    "ds_read_b64_tr_b16 v[56:57], v33 offset:0\n\t" \
    "v_mfma_f32_16x16x32_bf16 v[168:171], v[60:63], v[72:75], v[168:171]\n\t" \
    "buffer_load_dwordx4 v37, s[48:51], s63 offen lds\n\t" \
    "v_mfma_f32_16x16x32_bf16 v[172:175], v[60:63], v[76:79], v[172:175]\n\t" \
    "ds_read_b64_tr_b16 v[58:59], v33 offset:2048\n\t" \
    "s_waitcnt lgkmcnt(6)\n\t" \
    "v_mfma_f32_16x16x32_bf16 v[176:179], v[44:47], v[64:67], v[176:179]\n\t" \
    "ds_read_b64_tr_b16 v[60:61], v33 offset:8192\n\t" \
    "ds_read_b64_tr_b16 v[62:63], v33 offset:10240\n\t" \
    "v_mfma_f32_16x16x32_bf16 v[180:183], v[44:47], v[68:71], v[180:183]\n\t" \
    "s_add_u32 s60, s60, 131072\n\t" \
    "v_mfma_f32_16x16x32_bf16 v[184:187], v[44:47], v[72:75], v[184:187]\n\t" \
    "s_add_u32 s61, s61, s52\n\t" \
    "s_add_u32 s62, s62, s53\n\t" \
    "v_mfma_f32_16x16x32_bf16 v[188:191], v[44:47], v[76:79], v[188:191]\n\t" \
    "s_add_u32 s63, s63, s54\n\t" \
    "s_waitcnt lgkmcnt(6)\n\t" \
    "v_mfma_f32_16x16x32_bf16 v[192:195], v[48:51], v[64:67], v[192:195]\n\t" \
    "v_mfma_f32_16x16x32_bf16 v[196:199], v[48:51], v[68:71], v[196:199]\n\t" \
    "v_mfma_f32_16x16x32_bf16 v[200:203], v[48:51], v[72:75], v[200:203]\n\t" \
    "v_mfma_f32_16x16x32_bf16 v[204:207], v[48:51], v[76:79], v[204:207]\n\t" \
    "s_waitcnt vmcnt(10) lgkmcnt(0)\n\t" \
    "s_barrier\n\t" \
    "v_mfma_f32_16x16x32_bf16 v[208:211], v[52:55], v[64:67], v[208:211]\n\t" \
    "ds_read_b128 v[80:83], v34 offset:36864\n\t" \
    "ds_read_b64_tr_b16 v[44:45], v28 offset:36864\n\t" \
    "v_mfma_f32_16x16x32_bf16 v[212:215], v[52:55], v[68:71], v[212:215]\n\t" \
    "ds_read_b64_tr_b16 v[46:47], v28 offset:38912\n\t" \
    "v_mfma_f32_16x16x32_bf16 v[216:219], v[52:55], v[72:75], v[216:219]\n\t" \
    "ds_read_b128 v[84:87], v34 offset:37888\n\t" \
    "ds_read_b128 v[88:91], v34 offset:38912\n\t" \
    "v_mfma_f32_16x16x32_bf16 v[220:223], v[52:55], v[76:79], v[220:223]\n\t" \
    "ds_read_b128 v[92:95], v34 offset:39936\n\t" \
    "v_mfma_f32_16x16x32_bf16 v[224:227], v[56:59], v[64:67], v[224:227]\n\t" \
    "ds_read_b64_tr_b16 v[48:49], v29 offset:36864\n\t" \
    "v_mfma_f32_16x16x32_bf16 v[228:231], v[56:59], v[68:71], v[228:231]\n\t" \
    "ds_read_b64_tr_b16 v[50:51], v29 offset:38912\n\t" \
    "v_mfma_f32_16x16x32_bf16 v[232:235], v[56:59], v[72:75], v[232:235]\n\t" \
    "ds_read_b64_tr_b16 v[52:53], v30 offset:36864\n\t" \
    "v_mfma_f32_16x16x32_bf16 v[236:239], v[56:59], v[76:79], v[236:239]\n\t" \
    "ds_read_b64_tr_b16 v[54:55], v30 offset:38912\n\t" \
    "v_mfma_f32_16x16x32_bf16 v[240:243], v[60:63], v[64:67], v[240:243]\n\t" \
    "ds_read_b64_tr_b16 v[56:57], v30 offset:45056\n\t" \
    "v_mfma_f32_16x16x32_bf16 v[244:247], v[60:63], v[68:71], v[244:247]\n\t" \
    "v_mfma_f32_16x16x32_bf16 v[248:251], v[60:63], v[72:75], v[248:251]\n\t" \
    "ds_read_b64_tr_b16 v[58:59], v30 offset:47104\n\t" \
    "v_mfma_f32_16x16x32_bf16 v[252:255], v[60:63], v[76:79], v[252:255]\n\t" \
    "s_cmp_eq_u32 s64, 1\n\t" \
    "s_cbranch_scc0 LK2PF3_v1_%=\n\t" \
    "s_sub_u32 s55, s55, 73728\n\t" \
    "s_sub_u32 s56, s56, 73728\n\t" \
    "s_sub_u32 s58, s58, 73728\n\t" \
    "LK2PF3_v1_%=:\n\t" \
    "s_waitcnt lgkmcnt(6)\n\t" \
    "v_mfma_f32_16x16x32_bf16 v[96:99], v[44:47], v[80:83], v[96:99]\n\t" \
    "s_add_u32 m0, s58, 73728\n\t" \
    "v_mfma_f32_16x16x32_bf16 v[100:103], v[44:47], v[84:87], v[100:103]\n\t" \
    "ds_read_b64_tr_b16 v[60:61], v31 offset:36864\n\t" \
    "v_mfma_f32_16x16x32_bf16 v[104:107], v[44:47], v[88:91], v[104:107]\n\t" \
    "buffer_load_dwordx4 v38, s[36:39], s60 offen lds\n\t" \
    "v_mfma_f32_16x16x32_bf16 v[108:111], v[44:47], v[92:95], v[108:111]\n\t" \
    "ds_read_b64_tr_b16 v[62:63], v31 offset:38912\n\t" \
    "s_waitcnt lgkmcnt(6)\n\t" \
    "v_mfma_f32_16x16x32_bf16 v[112:115], v[48:51], v[80:83], v[112:115]\n\t" \
    "ds_read_b64_tr_b16 v[44:45], v31 offset:45056\n\t" \
    "v_mfma_f32_16x16x32_bf16 v[116:119], v[48:51], v[84:87], v[116:119]\n\t" \
    "buffer_load_dwordx4 v38, s[36:39], s60 offen offset:1024 lds\n\t" \
    "v_mfma_f32_16x16x32_bf16 v[120:123], v[48:51], v[88:91], v[120:123]\n\t" \
    "ds_read_b64_tr_b16 v[46:47], v31 offset:47104\n\t" \
    "v_mfma_f32_16x16x32_bf16 v[124:127], v[48:51], v[92:95], v[124:127]\n\t" \
    "s_waitcnt lgkmcnt(6)\n\t" \
    "v_mfma_f32_16x16x32_bf16 v[128:131], v[52:55], v[80:83], v[128:131]\n\t" \
    "s_add_u32 m0, s55, 73728\n\t" \
    "v_mfma_f32_16x16x32_bf16 v[132:135], v[52:55], v[84:87], v[132:135]\n\t" \
    "ds_read_b64_tr_b16 v[48:49], v32 offset:36864\n\t" \
    "v_mfma_f32_16x16x32_bf16 v[136:139], v[52:55], v[88:91], v[136:139]\n\t" \
    "buffer_load_dwordx4 v35, s[40:43], s61 offen lds\n\t" \
    "v_mfma_f32_16x16x32_bf16 v[140:143], v[52:55], v[92:95], v[140:143]\n\t" \
    "ds_read_b64_tr_b16 v[50:51], v32 offset:38912\n\t" \
    "s_waitcnt lgkmcnt(6)\n\t" \
    "v_mfma_f32_16x16x32_bf16 v[144:147], v[56:59], v[80:83], v[144:147]\n\t" \
    "s_add_u32 m0, s56, 73728\n\t" \
    "v_mfma_f32_16x16x32_bf16 v[148:151], v[56:59], v[84:87], v[148:151]\n\t" \
    "ds_read_b64_tr_b16 v[52:53], v32 offset:45056\n\t" \
    "v_mfma_f32_16x16x32_bf16 v[152:155], v[56:59], v[88:91], v[152:155]\n\t" \
    "buffer_load_dwordx4 v36, s[44:47], s62 offen lds\n\t" \
    "v_mfma_f32_16x16x32_bf16 v[156:159], v[56:59], v[92:95], v[156:159]\n\t" \
    "ds_read_b64_tr_b16 v[54:55], v32 offset:47104\n\t" \
    "s_waitcnt lgkmcnt(6)\n\t" \
    "v_mfma_f32_16x16x32_bf16 v[160:163], v[60:63], v[80:83], v[160:163]\n\t" \
    "s_add_u32 m0, s57, 0\n\t" \
    "v_mfma_f32_16x16x32_bf16 v[164:167], v[60:63], v[84:87], v[164:167]\n\t" \
    "ds_read_b64_tr_b16 v[56:57], v33 offset:36864\n\t" \
    "v_mfma_f32_16x16x32_bf16 v[168:171], v[60:63], v[88:91], v[168:171]\n\t" \
    "buffer_load_dwordx4 v37, s[48:51], s63 offen lds\n\t" \
    "v_mfma_f32_16x16x32_bf16 v[172:175], v[60:63], v[92:95], v[172:175]\n\t" \
    "ds_read_b64_tr_b16 v[58:59], v33 offset:38912\n\t" \
    "s_waitcnt lgkmcnt(6)\n\t" \
    "v_mfma_f32_16x16x32_bf16 v[176:179], v[44:47], v[80:83], v[176:179]\n\t" \
    "ds_read_b64_tr_b16 v[60:61], v33 offset:45056\n\t" \
    "ds_read_b64_tr_b16 v[62:63], v33 offset:47104\n\t" \
    "v_mfma_f32_16x16x32_bf16 v[180:183], v[44:47], v[84:87], v[180:183]\n\t" \
    "s_add_u32 s60, s60, 131072\n\t" \
    "v_mfma_f32_16x16x32_bf16 v[184:187], v[44:47], v[88:91], v[184:187]\n\t" \
    "s_add_u32 s61, s61, s52\n\t" \
    "s_add_u32 s62, s62, s53\n\t" \
    "v_mfma_f32_16x16x32_bf16 v[188:191], v[44:47], v[92:95], v[188:191]\n\t" \
    "s_add_u32 s63, s63, s54\n\t" \
    "s_waitcnt lgkmcnt(6)\n\t" \
    "v_mfma_f32_16x16x32_bf16 v[192:195], v[48:51], v[80:83], v[192:195]\n\t" \
    "v_subrev_u32 v28, 0x12000, v28\n\t" \
    "v_subrev_u32 v29, 0x12000, v29\n\t" \
    "v_mfma_f32_16x16x32_bf16 v[196:199], v[48:51], v[84:87], v[196:199]\n\t" \
    "v_subrev_u32 v30, 0x12000, v30\n\t" \
    "v_subrev_u32 v31, 0x12000, v31\n\t" \
    "v_mfma_f32_16x16x32_bf16 v[200:203], v[48:51], v[88:91], v[200:203]\n\t" \
    "v_subrev_u32 v32, 0x12000, v32\n\t" \
    "v_subrev_u32 v33, 0x12000, v33\n\t" \
    "v_mfma_f32_16x16x32_bf16 v[204:207], v[48:51], v[92:95], v[204:207]\n\t" \
    "v_subrev_u32 v34, 0x12000, v34\n\t" \
    "s_waitcnt vmcnt(10) lgkmcnt(0)\n\t" \
    "s_barrier\n\t" \
    "v_mfma_f32_16x16x32_bf16 v[208:211], v[52:55], v[80:83], v[208:211]\n\t" \
    "ds_read_b128 v[64:67], v34 offset:0\n\t" \
    "ds_read_b64_tr_b16 v[44:45], v28 offset:0\n\t" \
    "v_mfma_f32_16x16x32_bf16 v[212:215], v[52:55], v[84:87], v[212:215]\n\t" \
    "ds_read_b64_tr_b16 v[46:47], v28 offset:2048\n\t" \
    "v_mfma_f32_16x16x32_bf16 v[216:219], v[52:55], v[88:91], v[216:219]\n\t" \
    "ds_read_b128 v[68:71], v34 offset:1024\n\t" \
    "ds_read_b128 v[72:75], v34 offset:2048\n\t" \
    "v_mfma_f32_16x16x32_bf16 v[220:223], v[52:55], v[92:95], v[220:223]\n\t" \
    "ds_read_b128 v[76:79], v34 offset:3072\n\t" \
    "v_mfma_f32_16x16x32_bf16 v[224:227], v[56:59], v[80:83], v[224:227]\n\t" \
    "ds_read_b64_tr_b16 v[48:49], v29 offset:0\n\t" \
    "v_mfma_f32_16x16x32_bf16 v[228:231], v[56:59], v[84:87], v[228:231]\n\t" \
    "ds_read_b64_tr_b16 v[50:51], v29 offset:2048\n\t" \
    "v_mfma_f32_16x16x32_bf16 v[232:235], v[56:59], v[88:91], v[232:235]\n\t" \
    "ds_read_b64_tr_b16 v[52:53], v30 offset:0\n\t" \
    "v_mfma_f32_16x16x32_bf16 v[236:239], v[56:59], v[92:95], v[236:239]\n\t" \
    "ds_read_b64_tr_b16 v[54:55], v30 offset:2048\n\t" \
    "v_mfma_f32_16x16x32_bf16 v[240:243], v[60:63], v[80:83], v[240:243]\n\t" \
    "ds_read_b64_tr_b16 v[56:57], v30 offset:8192\n\t" \
    "v_mfma_f32_16x16x32_bf16 v[244:247], v[60:63], v[84:87], v[244:247]\n\t" \
    "v_mfma_f32_16x16x32_bf16 v[248:251], v[60:63], v[88:91], v[248:251]\n\t" \
    "ds_read_b64_tr_b16 v[58:59], v30 offset:10240\n\t" \
    "v_mfma_f32_16x16x32_bf16 v[252:255], v[60:63], v[92:95], v[252:255]\n\t" \
    "s_sub_u32 s64, s64, 1\n\t" \
    "s_cmp_lg_u32 s64, 0\n\t" \
    "s_cbranch_scc1 LK2_v1_%=\n\t" \
    "LK2END_%=:\n\t" \
    "s_waitcnt lgkmcnt(0)\n\t" \
    "s_barrier\n\t" \
    "s_nop 7\n\t" \
    "s_nop 7\n\t"

extern "C" __global__ __launch_bounds__(512, 2) void k2_gemm1(K2Args A) {
  extern __shared__ __attribute__((aligned(16))) char lds[];
  const int lane = threadIdx.x & 63;
  const int wid = rfl(threadIdx.x >> 6), cg = wid >> 2, tg = wid & 3;
  const int bid = blockIdx.x, xcd = bid & 7, idx = bid >> 3, panel = idx & 7;
  const int pr = MIX ? 2 * xcd + (idx >> 4) : (xcd & 3) * 4 + (idx >> 3), isY = MIX ? (idx >> 3) & 1 : xcd >> 2;
  const int rt3 = MIX ? (xcd >> 1) * 4 + (idx & 3) : (xcd & 3) * 4 + (idx & 3);
  const unsigned lb = (unsigned)(size_t)lds;
  char* ws = A.ws;
  const u16 *wqkv = A.wqkv, *wphi = A.wphi, *wg = A.wg, *wu = A.wu;
  u32x16 sg;
  u32x8 sp;
  u32x4 sq;
  const unsigned scr = rfl(lb + wid * 1024);
  u32x16 vg;
  const char* pfp; unsigned pfon;
  {
    const unsigned long long xp = (unsigned long long)(ws + WS_XN);
    sg[0] = (unsigned)xp; sg[1] = (unsigned)(xp >> 32) & 0xffffu; sg[2] = K2_NOX ? 0u : 4u << 20; sg[3] = 0x00020000u;
    const int g = lane >> 4, q4 = (lane >> 2) & 3, p4 = lane & 3;
    const int rowA = tg * 8 + (lane >> 3);
    const int inA = ((((lane & 7) >> 1) ^ ((rowA >> 1) & 3)) * 32) + (lane & 1) * 16;
    const int kr = 4 * g + q4, x4 = 2 * (g & 1) + (q4 >> 1);
    const unsigned r128 = lb + 16384 + kr * 128 + 8 * p4;
    vg[0] = r128 + (((2 * cg) ^ x4) * 32); vg[1] = r128 + (((2 * cg + 1) ^ x4) * 32);
    if (!isY) {
      if (cg == 0) {
        const int u0 = tg * 64 + lane, r0 = (u0 * 5462) >> 16, c0 = u0 - r0 * 12;
        const int o0 = r0 * 7168 + 192 * pr + (((c0 >> 1) ^ ((r0 >> 2) & 1)) * 32) + (c0 & 1) * 16;
        const int u2 = (4 + (tg & 1)) * 64 + lane, r2 = (u2 * 5462) >> 16, c2 = u2 - r2 * 12;
        const int o2 = r2 * 7168 + 192 * pr + (((c2 >> 1) ^ ((r2 >> 2) & 1)) * 32) + (c2 & 1) * 16;
        K2_SET_SLOT(0, wg, 7168, 8 + tg, o0);
        K2_SET_SLOT(1, wu, 7168, 14 + tg, o0);
        K2_SET_SLOT(2, tg < 2 ? wg : wu, 7168, (tg < 2 ? 12 : 18) + (tg & 1), o2);
      } else {
        const int o = rowA * 6144 + 2048 + 128 * pr + inA;
        K2_SET_SLOT(0, wqkv, 6144, tg, o);
        K2_SET_SLOT(1, wqkv, 6144, 4 + tg, o + 2048);
        K2_TOUCH_SLOT(8, 6, 2048 + 128 * pr, 192 * pr);
      }
      const unsigned r192 = lb + 16384 + 8192 + kr * 192 + 8 * p4;
      vg[2] = r192 + (g & 1) * 32; vg[3] = r192 - (g & 1) * 32; vg[4] = 0; vg[5] = 0;
    } else {
      const int og = rowA * 7168 + (1536 + 128 * pr) * 2 + inA;
      if (cg == 0) {
        K2_SET_SLOT(0, wg, 7168, 8 + tg, og + 128);
        K2_SET_SLOT(1, wu, 7168, 12 + tg, og);
        K2_SET_SLOT(2, wu, 7168, 16 + tg, og + 128);
      } else {
        K2_SET_SLOT(0, wqkv, 6144, tg, rowA * 6144 + 128 * pr + inA);
        K2_SET_SLOT(1, wg, 7168, 4 + tg, og);
        K2_TOUCH_SLOT(4, 8, 128 * pr, (1536 + 128 * pr) * 2);
      }
      const unsigned rY = r128 + 4096 + cg * 4096;
      vg[2] = rY + ((0 ^ x4) * 32); vg[3] = rY + ((1 ^ x4) * 32); vg[4] = rY + ((2 ^ x4) * 32); vg[5] = rY + ((3 ^ x4) * 32);
    }
    sp[6] = lb + wid * 2048; sp[7] = cg;
    vg[6] = lb + tg * 4096 + lane * 16;
    vg[10] = panel * 16384 + wid * 2048 + lane * 16;
    vg[11] = (bid * 8 + wid) * 1024 + lane * 16; vg[12] = (bid * 4 + tg) * 1024 + lane * 16;
#pragma unroll
    for (int i = 13; i < 16; ++i) vg[i] = 0;
    { const unsigned long long pd = (unsigned long long)A.wd, po = (unsigned long long)A.wo;
      sq[0] = rfl((unsigned)pd); sq[1] = rfl((unsigned)(pd >> 32) & 0xffffu); sq[2] = rfl((unsigned)po); sq[3] = rfl((unsigned)(po >> 32) & 0xffffu); }
#pragma unroll
    for (int i = 0; i < 16; ++i) sg[i] = rfl(sg[i]);
#pragma unroll
    for (int i = 0; i < 8; ++i) sp[i] = rfl(sp[i]);
    {
      const int gw = idx * 8 + wid;
      int u = (gw - 64) * 64 + lane;
      const char* p = ws + WS_A2;
      int n = 400;
      if (u >= 400) { u -= 400; p = (const char*)A.wd; n = 112;
        if (u >= 112) { u -= 112; p = (const char*)A.wo; n = 32;
          if (u >= 32) { u -= 32; p = (const char*)A.out; n = 64; } } }
      if (u >= n || u < 0) u = 0;
      pfp = p + (size_t)u * 65536;
      pfon = rfl(gw >= 64 && gw < 74);
    }
    if (cg == 0) {
      const int hh = tg >> 1, pc = tg & 1, o = pc * 1024 + lane * 16;
      const int oct = o >> 9, c = (o >> 8) & 1, r8 = (o >> 5) & 7, hf = (o >> 4) & 1;
      const u16* src = wphi + (size_t)(2 * pr + hh) * 1024 + (oct * 8 + r8) * 32 + c * 16 + hf * 8;
      const unsigned m0v = rfl(lb + K2_WPHI + hh * 2048 + pc * 1024);
      asm volatile("s_mov_b32 m0, %1\n\ts_nop 0\n\tglobal_load_lds_dwordx4 %0, off" :: "v"(src), "s"(m0v) : "memory");
    }
  }

  unsigned hseen;
  if (!isY) {
    K2_ASM(K2_LOOP_X)
    const unsigned pk = lb + 49152 + cg * 49152, vv = pk + 32768;
    const int t = lane & 15;
#pragma unroll
    for (int tt = 0; tt < 4; ++tt) {
      const int tok = tg * 64 + tt * 16 + t;
      const unsigned vo = vv + (tok >> 3) * 512 + (tok & 7) * 32 + 8 * (g ^ (t >> 2));
      const f32x4 v0 = sub4(a2, tt), v1 = sub4(a3, tt);
      *(LAS u32x2*)(size_t)(vo) = (u32x2){cvtpk(v0[0], v0[1]), cvtpk(v0[2], v0[3])};
      *(LAS u32x2*)(size_t)(vo + 256) = (u32x2){cvtpk(v1[0], v1[1]), cvtpk(v1[2], v1[3])};
    }
    __builtin_amdgcn_sched_barrier(0);
#pragma unroll
    for (int tp = 0; tp < 2; ++tp) {
      Phi ph[2];
      feature_map2<true>(sub4(a0, 2 * tp), sub4(a1, 2 * tp), sub4(a0, 2 * tp + 1), sub4(a1, 2 * tp + 1), w0, w1, ph[0], ph[1]);
#pragma unroll
      for (int i = 0; i < 2; ++i) {
        const int tt = 2 * tp + i, tok = tg * 64 + tt * 16 + t;
        const unsigned ro = pk + (tok >> 3) * 1024 + (tok & 7) * 32 + 8 * (g ^ (t >> 2));
        *(LAS u32x2*)(size_t)(ro) = (u32x2){ph[i].p[0], ph[i].p[1]};
        *(LAS u32x2*)(size_t)(ro + 256) = (u32x2){ph[i].p[2], ph[i].p[3]};
        *(LAS u32x2*)(size_t)(ro + 512) = (u32x2){ph[i].n[0], ph[i].n[1]};
        *(LAS u32x2*)(size_t)(ro + 768) = (u32x2){ph[i].n[2], ph[i].n[3]};
      }
      __builtin_amdgcn_sched_barrier(0);
    }
    __syncthreads();
    f32x4 kv0 = {0.f, 0.f, 0.f, 0.f}, kv1 = kv0, zz = kv0;
    const bf16x8 ones = mk8(0x3f803f80u, 0x3f803f80u, 0x3f803f80u, 0x3f803f80u);
    {
      const unsigned trx = (4 * (g & 1) + q4) * 32 + 8 * (p4 ^ g);
      const unsigned pa = pk + (g >> 1) * 1024 + tg * 256 + trx, va = vv + (g >> 1) * 512 + trx;
      bf16x8 fa[8], b0[8], b1[8];
#pragma unroll
      for (int b = 0; b < 8; ++b) {
        fa[b] = tr_frag(pa + b * 4096, pa + b * 4096 + 2048);
        b0[b] = tr_frag(va + b * 2048, va + b * 2048 + 1024); b1[b] = tr_frag(va + b * 2048 + 256, va + b * 2048 + 256 + 1024);
      }
      __builtin_amdgcn_sched_barrier(0);
#pragma unroll
      for (int b = 0; b < 8; ++b) { kv0 = MFMA16(fa[b], b0[b], kv0); kv1 = MFMA16(fa[b], b1[b], kv1); zz = MFMA16(fa[b], ones, zz); }
    }
    {
      const int u = (panel * 32 + head) * 4 + tg;
      __builtin_amdgcn_raw_buffer_store_b128((u32x4){cvtpk(kv0[0], kv0[1]), cvtpk(kv0[2], kv0[3]), cvtpk(kv1[0], kv1[1]), cvtpk(kv1[2], kv1[3])}, rK, u * 1024 + lane * 16, 0, 16);
      if (t < 4) __builtin_amdgcn_raw_buffer_store_b64((u32x2){cvtpk(zz[0], zz[1]), cvtpk(zz[2], zz[3])}, rK, (1 << 20) + u * 128 + g * 32 + t * 8, 0, 16);
    }
    __builtin_amdgcn_sched_barrier(0);
    hseen = __hip_atomic_load(flags + FL_HY + 32 * (rt3 >> 1), __ATOMIC_RELAXED, __HIP_MEMORY_SCOPE_AGENT);
#pragma unroll
    for (int tt = 0; tt < 4; ++tt) {
      const u32x2 h0 = geglu4(sub4(a4, tt), sub4(a5, tt)), h1 = geglu4(sub4(a6, tt), sub4(a7, tt)), h2 = geglu4(sub4(a8, tt), sub4(a9, tt));
      const unsigned ob = (unsigned)(32 + 3 * pr) * 131072u + (tt0 + tt) * 1024 + lane * 16;
      if (cg == 0) {
        __builtin_amdgcn_raw_buffer_store_b128((u32x4){h0[0], h0[1], h1[0], h1[1]}, rA2, ob, 0, A2_AUX);
        __builtin_amdgcn_raw_buffer_store_b64(h2, rA2, ob + 131072, 0, A2_AUX);
      } else {
        __builtin_amdgcn_raw_buffer_store_b64(h0, rA2, ob + 131072 + 8, 0, A2_AUX);
        __builtin_amdgcn_raw_buffer_store_b128((u32x4){h1[0], h1[1], h2[0], h2[1]}, rA2, ob + 2 * 131072, 0, A2_AUX);
      }
      __builtin_amdgcn_sched_barrier(0);
    }
  } else {
    K2_ASM(K2_LOOP_Y)
    const unsigned hb = (unsigned)(32 + 48 + 4 * pr + 2 * cg) * 131072u + tt0 * 1024 + lane * 16;
#pragma unroll
    for (int tt = 0; tt < 4; ++tt) {
      const u32x2 h0 = geglu4(sub4(a2, tt), sub4(a3, tt)), h1 = geglu4(sub4(a4, tt), sub4(a5, tt));
      __builtin_amdgcn_raw_buffer_store_b128((u32x4){h0[0], h0[1], h1[0], h1[1]}, rA2, hb + tt * 1024, 0, A2_AUX);
      __builtin_amdgcn_sched_barrier(0);
    }
#pragma unroll
    for (int tt = 0; tt < 4; ++tt) {
      const u32x2 h2 = geglu4(sub4(a6, tt), sub4(a7, tt)), h3 = geglu4(sub4(a8, tt), sub4(a9, tt));
      __builtin_amdgcn_raw_buffer_store_b128((u32x4){h2[0], h2[1], h3[0], h3[1]}, rA2, hb + 131072 + tt * 1024, 0, A2_AUX);
      __builtin_amdgcn_sched_barrier(0);
    }
    Phi ph[4];
    feature_map2<false>(sub4(a0, 0), sub4(a1, 0), sub4(a0, 1), sub4(a1, 1), w0, w1, ph[0], ph[1]);
    __builtin_amdgcn_sched_barrier(0);
    feature_map2<false>(sub4(a0, 2), sub4(a1, 2), sub4(a0, 3), sub4(a1, 3), w0, w1, ph[2], ph[3]);
    __builtin_amdgcn_sched_barrier(0);
    float rp[4], rn[4];
    {
      const bf16x8 ones = mk8(0x3f803f80u, 0x3f803f80u, 0x3f803f80u, 0x3f803f80u);
      const f32x4 z = {0.f, 0.f, 0.f, 0.f};
#pragma unroll
      for (int tt = 0; tt < 4; ++tt) {
        const f32x4 sp_ = MFMA16(ones, mk8(ph[tt].p[0], ph[tt].p[1], ph[tt].p[2], ph[tt].p[3]), z);
        const f32x4 sn_ = MFMA16(ones, mk8(ph[tt].n[0], ph[tt].n[1], ph[tt].n[2], ph[tt].n[3]), z);
        rp[tt] = __builtin_amdgcn_rcpf(sp_[0]); rn[tt] = __builtin_amdgcn_rcpf(sn_[0]);
      }
    }
    asm volatile("s_waitcnt vmcnt(0)" ::: "memory");
    __syncthreads();
    if (tid == 0) __hip_atomic_fetch_add(flags + FL_HY + 32 * panel, 1u, __ATOMIC_RELAXED, __HIP_MEMORY_SCOPE_AGENT);
    int tid3 = threadIdx.x; asm volatile("" : "+v"(tid3));
    const int ln = tid3 & 63, gg = ln >> 4;
    __builtin_amdgcn_sched_barrier(0);
    u32x4 kp[8]; u32x2 zp[8];
    const int u0 = head * 4 + tg;
#pragma unroll
    for (int pn = 0; pn < 8; ++pn) {
      kp[pn] = __builtin_amdgcn_raw_buffer_load_b128(rK, (pn * 128 + u0) * 1024 + ln * 16, 0, 16);
      zp[pn] = __builtin_amdgcn_raw_buffer_load_b64(rK, (1 << 20) + (pn * 128 + u0) * 128 + gg * 32, 0, 16);
    }
    for (unsigned spins = 0;;) {
      unsigned miss = 0;
#pragma unroll
      for (int pn = 0; pn < 8; ++pn)
        miss |= __any(kp[pn][0] == SENT || kp[pn][1] == SENT || kp[pn][2] == SENT || kp[pn][3] == SENT || zp[pn][0] == SENT || zp[pn][1] == SENT) ? 1u << pn : 0u;
      if (miss == 0 || (++spins > 16u && spin_give_up(spins, tmo))) break;
#pragma unroll
      for (int pn = 0; pn < 8; ++pn)
        if (miss >> pn & 1u) {
          kp[pn] = __builtin_amdgcn_raw_buffer_load_b128(rK, (pn * 128 + u0) * 1024 + ln * 16, 0, 16);
          zp[pn] = __builtin_amdgcn_raw_buffer_load_b64(rK, (1 << 20) + (pn * 128 + u0) * 128 + gg * 32, 0, 16);
        }
    }
    {
      f32x2 s[4], sz[2];
#pragma unroll
      for (int i = 0; i < 4; ++i) s[i] = (f32x2){bflo(kp[0][i]), bfhi(kp[0][i])};
#pragma unroll
      for (int i = 0; i < 2; ++i) sz[i] = (f32x2){bflo(zp[0][i]), bfhi(zp[0][i])};
#pragma unroll
      for (int pn = 1; pn < 8; ++pn) {
#pragma unroll
        for (int i = 0; i < 4; ++i) s[i] += (f32x2){bflo(kp[pn][i]), bfhi(kp[pn][i])};
#pragma unroll
        for (int i = 0; i < 2; ++i) sz[i] += (f32x2){bflo(zp[pn][i]), bfhi(zp[pn][i])};
      }
      const unsigned kb = lb + K2_KVF + cg * 6144 + tg * 3 * 512 + ln * 8;
      *(LAS u32x2*)(size_t)(kb) = (u32x2){cvtpk(s[0][0], s[0][1]), cvtpk(s[1][0], s[1][1])};
      *(LAS u32x2*)(size_t)(kb + 512) = (u32x2){cvtpk(s[2][0], s[2][1]), cvtpk(s[3][0], s[3][1])};
      *(LAS u32x2*)(size_t)(kb + 1024) = (u32x2){cvtpk(sz[0][0], sz[0][1]), cvtpk(sz[1][0], sz[1][1])};
    }
    __syncthreads();
    hseen = __hip_atomic_load(flags + FL_HX + 32 * (rt3 >> 1), __ATOMIC_RELAXED, __HIP_MEMORY_SCOPE_AGENT);
    bf16x8 kf[2][3];
#pragma unroll
    for (int sb = 0; sb < 2; ++sb)
#pragma unroll
      for (int u = 0; u < 3; ++u) {
        const unsigned kb = lb + K2_KVF + cg * 6144 + u * 512 + ln * 8;
        const u32x2 lo = *(LAS u32x2*)(size_t)(kb + (2 * sb) * 1536), hi = *(LAS u32x2*)(size_t)(kb + (2 * sb + 1) * 1536);
        kf[sb][u] = mk8(lo[0], lo[1], hi[0], hi[1]);
      }
#pragma unroll
    for (int tt = 0; tt < 4; ++tt) {
      const f32x4 z = {0.f, 0.f, 0.f, 0.f};
      const bf16x8 fp = mk8(ph[tt].p[0], ph[tt].p[1], ph[tt].p[2], ph[tt].p[3]), fn = mk8(ph[tt].n[0], ph[tt].n[1], ph[tt].n[2], ph[tt].n[3]);
      const f32x4 p0 = MFMA16(kf[0][0], fp, z), p1 = MFMA16(kf[0][1], fp, z), pd = MFMA16(kf[0][2], fp, z);
      const f32x4 n0 = MFMA16(kf[1][0], fn, z), n1 = MFMA16(kf[1][1], fn, z), nd = MFMA16(kf[1][2], fn, z);
      const float r = __builtin_amdgcn_rcpf(pd[0] * rp[tt] + nd[0] * rn[tt] + 1e-6f);
      const float cp = rp[tt] * r, cn = rn[tt] * r;
      const f32x4 o0 = p0 * cp + n0 * cn, o1 = p1 * cp + n1 * cn;
      __builtin_amdgcn_raw_buffer_store_b128((u32x4){cvtpk(o0[0], o0[1]), cvtpk(o0[2], o0[3]), cvtpk(o1[0], o1[1]), cvtpk(o1[2], o1[3])},
                                             rA2, (unsigned)head * 131072u + (tt0 + tt) * 1024 + ln * 16, 0, A2_AUX);
    }
  }
  k3_phase(A.x, A.wo, A.wd, A.out, ws, lb, hseen);
}

extern "C" void kernel_launch(void* const* d_in, const int* in_sizes, int n_in, void* d_out, int out_size, void* d_ws, size_t ws_size,
                              hipStream_t stream) {
  const u16* x = (const u16*)d_in[0];
  char* ws = (char*)d_ws;
  (void)hipFuncSetAttribute((const void*)k2_gemm1, hipFuncAttributeMaxDynamicSharedMemorySize, K2_LDS);
  K1Args a1{x, (const u16*)d_in[1], (const u16*)d_in[2], (const u16*)d_in[5], (const u16*)d_in[6], (const u16*)d_in[3], (const u16*)d_in[7], (const u16*)d_out, ws};
  k1_ln<<<256 + 32, 512, 0, stream>>>(a1);
  K2Args a2{(const u16*)d_in[1], (const u16*)d_in[2], (const u16*)d_in[5], (const u16*)d_in[6], (const u16*)d_in[3], (const u16*)d_in[7], x, (u16*)d_out, ws};
  k2_gemm1<<<256, 512, K2_LDS, stream>>>(a2);
}
```
